```python
import torch

def _softmax(x, axis=-1):
    return torch.softmax(x, dim=axis)
def _gelu(x):
    return torch.nn.functional.gelu(x, approximate="tanh")
def _silu(x):
    return torch.nn.functional.silu(x)
def _sigmoid(x):
    return torch.sigmoid(x)
def _layernorm(x, axis=-1, eps=1e-5):
    m = x.mean(dim=axis, keepdim=True)
    v = x.var(dim=axis, keepdim=True, unbiased=False)
    return (x - m) / torch.sqrt(v + eps)
def _scatter_add(tgt, idx, src, axis=0):
    return tgt.clone().index_add_(axis, idx, src)
def _segment_sum(src, seg, n):
    out = torch.zeros((n,) + src.shape[1:], dtype=src.dtype)
    return out.index_add_(0, seg, src)
def _pad(x, pw):
    flat = tuple(v for pair in reversed(pw) for v in pair)
    return torch.nn.functional.pad(x, flat)

kernel_name = 'recurrent_scan__sliding_block_cached__swiglu_ffn__decode'

def setup_inputs():

    _w = (lambda a: (lambda t: t.to(torch.bfloat16) if t.is_floating_point() else t)(torch.as_tensor(a)))
    def _A_setup():
        B, Sq, D = 1, 1, 576
        Hq, Hkv, Sk, Dh = 32, 16, 65536, 128
        assert Hq % Hkv == 0, (Hq, Hkv)
        return {
            "x": _w((lambda *s: torch.randn(*s, dtype=torch.bfloat16) * 0.05)(B, Sq, D)),
            "k_cache": _w((lambda *s: torch.randn(*s, dtype=torch.bfloat16) * 0.8)(B, Hkv, Sk, Dh)),
            "v_cache": _w((lambda *s: torch.randn(*s, dtype=torch.bfloat16) * 0.2)(B, Hkv, Sk, Dh)),
            "wq": _w((lambda *s: torch.randn(*s, dtype=torch.bfloat16) * 0.8)(D, Hq * Dh) * (D ** -0.5)),
            "wo": _w((lambda *s: torch.randn(*s, dtype=torch.bfloat16) * 0.2)(Hq * Dh, D) * ((Hq * Dh) ** -0.5)),
        }
    _A_in = _A_setup()

    def _B_setup():
        B, S, D, F = 1, 1, 576, 4608
        return {
            "x": _w((lambda *s: torch.randn(*s, dtype=torch.bfloat16) * 0.05)(B, S, D)),
            "w_gate": _w((lambda *s: torch.randn(*s, dtype=torch.bfloat16) * 0.1)(D, F)),
            "w_up": _w((lambda *s: torch.randn(*s, dtype=torch.bfloat16) * 0.1)(D, F)),
            "w_down": _w((lambda *s: torch.randn(*s, dtype=torch.bfloat16) * 0.1)(F, D)),
        }
    _B_in = _B_setup()

    return {
        "A_x": _A_in["x"],
        "A_k_cache": _A_in["k_cache"],
        "A_v_cache": _A_in["v_cache"],
        "A_wq": _A_in["wq"],
        "A_wo": _A_in["wo"],
        "B_x": _B_in["x"],
        "B_w_gate": _B_in["w_gate"],
        "B_w_up": _B_in["w_up"],
        "B_w_down": _B_in["w_down"],
    }

def reference(A_x, A_k_cache, A_v_cache, A_wq, A_wo, B_x, B_w_gate, B_w_up, B_w_down):
    def _A_ref(x, k_cache, v_cache, wq, wo):
        B, Sq, D = x.shape
        Hq, Dh = 32, 128
        Hkv, Sk = k_cache.shape[1], k_cache.shape[2]
        W = min(4096, max(256, Sk // 4))
        rep = Hq // Hkv
        q = (lambda x, *d: x.permute(*d))((x @ wq).reshape(B, Sq, Hq, Dh), 0, 2, 1, 3)
        k = (lambda x, n, axis: torch.repeat_interleave(x, n, dim=axis))(k_cache[:, :, Sk - W:, :], rep, axis=1)
        v = (lambda x, n, axis: torch.repeat_interleave(x, n, dim=axis))(v_cache[:, :, Sk - W:, :], rep, axis=1)
        scores = torch.einsum("bhqd,bhkd->bhqk", q, k) * (Dh ** -0.5)
        p = _softmax(scores, axis=-1)
        o = torch.einsum("bhqk,bhkd->bhqd", p, v)
        return (lambda x, *d: x.permute(*d))(o, 0, 2, 1, 3).reshape(B, Sq, Hq * Dh) @ wo

    def _B_ref(x, w_gate, w_up, w_down):
        g = _silu(x @ w_gate)
        u = x @ w_up
        return (g * u) @ w_down

    x = A_x
    xn = _layernorm(x, axis=-1)
    s = _A_ref(xn, A_k_cache, A_v_cache, A_wq, A_wo)
    g = _silu(_B_ref(xn, B_w_gate, B_w_up, B_w_down))
    return x + s * g
```

```cpp
#include <hip/hip_runtime.h>
#include <hip/hip_bf16.h>
#include <stdint.h>

#define GAS __attribute__((address_space(1)))
#define LAS __attribute__((address_space(3)))
typedef unsigned v4u __attribute__((ext_vector_type(4)));
typedef unsigned v2u __attribute__((ext_vector_type(2)));
typedef float f32x4 __attribute__((ext_vector_type(4)));
typedef float f32x2 __attribute__((ext_vector_type(2)));
typedef short bf16x8 __attribute__((ext_vector_type(8)));
typedef __bf16 bf16x2 __attribute__((ext_vector_type(2)));
typedef unsigned short u16;
typedef __amdgpu_buffer_rsrc_t rsrc_t;

#define KWIN 4
#define DT1 34
#define DT2 36
#define DTD 36
#define DTO 36
#define TSTART 10
#define QKPOS 6
#define QKG 3
#define W9GAP 70
#define T_LAST 790
#define CODE_BYTES 17408
#define CODE_PF_WGS 2
#define TB_READ 170
#define TB_MAX 300
#define DLY_Q 36
#define DLY_O 30
#define DLY_H 20
#define DLY_F 20
#define DLY_S 28
#define RETRY 8

namespace {

constexpr int D = 576, SK = 65536, DH = 128, WIN = 4096, F = 4608, NQ = 4096;
constexpr int NWG = 256, NWAVE = 11, NTHR = NWAVE * 64;
constexpr int LDS_BYTES = 84 * 1024;

constexpr int WS_EPOCH = 0;
constexpr int WS_TB = 256;
constexpr int WS_QP = WS_TB + 1024;
constexpr int WS_OP = WS_QP + 16 * 4096;
constexpr int WS_GP = WS_OP + 16 * 16 * 1152;
constexpr int WS_SP = WS_GP + 8 * 32 * 2304;
constexpr int WS_FP = WS_SP + 256 * 2304;
constexpr int WS_END = WS_FP + 256 * 2304;

constexpr int L_FLAG = 0;
constexpr int L_OS = 64;
constexpr int L_HS = L_OS + 64;
constexpr int L_XQ = L_HS + 128;
constexpr int L_XG = L_XQ + 576;
constexpr int L_XR = L_XG + 160;
constexpr int L_QB = L_XR + 16;
constexpr int L_ML = L_QB + 512;
constexpr int L_FIN = L_ML + 128;
constexpr int L_PO = L_FIN + 16;
constexpr int L_X = L_PO + 8192;
constexpr int L_WQ = L_X + 2048;
constexpr int L_TS = L_WQ + 18 * 1024;
static_assert(L_PO % 16 == 0 && L_XQ % 16 == 0 && L_XG % 16 == 0 && L_QB % 16 == 0 && L_OS % 16 == 0 && L_HS % 16 == 0 && L_XR % 16 == 0 && L_X % 16 == 0, "align");
enum { F_XN = 0, F_Q = 1, C_ATT = 2, C_GU = 3, F_OP = 4, C_SP = 5, C_FP = 6, F_S = 7, W_TAG = 8, W_SHIFT = 9, F_X = 10, F_WQ = 11 };

constexpr int AUX_NT = 2, AUX_SC1 = 16;
constexpr unsigned VMASK = 0xFFFFFC00u, TMASK = 0x3FFu, OOB = 0xFFFFFF00u;
constexpr unsigned EMPTY = 0xFFFF0000u;
constexpr unsigned SPIN_CAP = 1u << 17;

struct Params {
  const u16* x; const u16* kc; const u16* vc; const u16* wq; const u16* wo;
  const u16* wg; const u16* wu; const u16* wd; u16* out; unsigned char* ws;
};

__device__ __forceinline__ unsigned fbits(float f) { return __builtin_bit_cast(unsigned, f); }
__device__ __forceinline__ float asf(unsigned u) { return __builtin_bit_cast(float, u); }
__device__ __forceinline__ unsigned cvtpk(float a, float b) { bf16x2 r = {(__bf16)a, (__bf16)b}; return __builtin_bit_cast(unsigned, r); }
__device__ __forceinline__ float rbf(float f) { return asf(cvtpk(0.f, f) & 0xffff0000u); }
__device__ __forceinline__ float silu_f(float x) { return x * __builtin_amdgcn_rcpf(1.f + __builtin_amdgcn_exp2f(x * -1.4426950408889634f)); }
__device__ __forceinline__ f32x4 mfma(v4u a, v4u b, f32x4 c) {
  return __builtin_amdgcn_mfma_f32_16x16x32_bf16(__builtin_bit_cast(bf16x8, a), __builtin_bit_cast(bf16x8, b), c, 0, 0, 0);
}
template <int CTRL> __device__ __forceinline__ float dpp(float x) {
  return asf(__builtin_amdgcn_mov_dpp(__builtin_bit_cast(int, x), CTRL, 0xf, 0xf, true));
}
template <int CTRL, int RM> __device__ __forceinline__ float dppm(float x) {
  return asf(__builtin_amdgcn_update_dpp(0, __builtin_bit_cast(int, x), CTRL, RM, 0xf, true));
}
constexpr int XOR1 = 0xB1, XOR2 = 0x4E, XOR7 = 0x141, ROR8 = 0x128;
__device__ __forceinline__ float x16_max(float x) {
  auto s = __builtin_amdgcn_permlane16_swap(fbits(x), fbits(x), false, false);
  x = fmaxf(asf(s[0]), asf(s[1]));
  auto t = __builtin_amdgcn_permlane32_swap(fbits(x), fbits(x), false, false);
  return fmaxf(asf(t[0]), asf(t[1]));
}
__device__ __forceinline__ float x16_sum(float x) {
  auto s = __builtin_amdgcn_permlane16_swap(fbits(x), fbits(x), false, false);
  x = asf(s[0]) + asf(s[1]);
  auto t = __builtin_amdgcn_permlane32_swap(fbits(x), fbits(x), false, false);
  return asf(t[0]) + asf(t[1]);
}
__device__ __forceinline__ float row_sum16(float x) { x += dpp<XOR1>(x); x += dpp<XOR2>(x); x += dpp<XOR7>(x); x += dpp<ROR8>(x); return x; }
__device__ __forceinline__ float row_max16(float x) { x = fmaxf(x, dpp<XOR1>(x)); x = fmaxf(x, dpp<XOR2>(x)); x = fmaxf(x, dpp<XOR7>(x)); x = fmaxf(x, dpp<ROR8>(x)); return x; }
__device__ __forceinline__ float wave_sum_s(float s) {
  s += dpp<0x111>(s); s += dpp<0x112>(s); s += dpp<0x114>(s); s += dpp<0x118>(s);
  s += dppm<0x142, 0xa>(s); s += dppm<0x143, 0xc>(s);
  return asf(__builtin_amdgcn_readlane(fbits(s), 63));
}
__device__ __forceinline__ float tr_sum16(f32x4 v, int lane) {
  const bool b3 = lane & 8, b2 = lane & 4;
  const float a = (b3 ? v[2] : v[0]) + dpp<ROR8>(b3 ? v[0] : v[2]);
  const float b = (b3 ? v[3] : v[1]) + dpp<ROR8>(b3 ? v[1] : v[3]);
  float r = (b2 ? b : a) + dpp<XOR7>(b2 ? a : b);
  r += dpp<XOR2>(r); r += dpp<XOR1>(r);
  return r;
}

__device__ __forceinline__ rsrc_t mk_rsrc(const void* p, unsigned bytes) {
  return __builtin_amdgcn_make_buffer_rsrc((void*)p, 0, (int)bytes, 0x00020000);
}
__device__ __forceinline__ v4u ld_nt(rsrc_t r, unsigned off) { return __builtin_amdgcn_raw_buffer_load_b128(r, (int)off, 0, AUX_NT); }
__device__ __forceinline__ v4u ld_sc1(rsrc_t r, unsigned off) { return __builtin_amdgcn_raw_buffer_load_b128(r, (int)off, 0, AUX_SC1); }
__device__ __forceinline__ void st_sc1(rsrc_t r, unsigned off, v4u g) { __builtin_amdgcn_raw_buffer_store_b128(g, r, (int)off, 0, AUX_SC1); }
__device__ __forceinline__ unsigned now32() { unsigned long long t; asm volatile("s_memrealtime %0\n\ts_waitcnt lgkmcnt(0)" : "=s"(t) :: "memory"); return (unsigned)t; }
__device__ __forceinline__ void wait_until(unsigned t) {
#pragma nounroll
  while ((int)(now32() - t) < 0) __builtin_amdgcn_s_sleep(1);
}
#define PIN() __builtin_amdgcn_sched_barrier(0)
#define CBAR() asm volatile("" ::: "memory")
#define WAIT_VM(n) asm volatile("s_waitcnt vmcnt(%0)" ::"i"(n) : "memory")
#define WAIT_LGKM0() asm volatile("s_waitcnt lgkmcnt(0)" ::: "memory")
#define UNI(x) __builtin_amdgcn_readfirstlane(x)

struct Tag { unsigned t0, t1, t2, t3; };
__device__ __forceinline__ Tag mk_tag(unsigned T) { return Tag{T & TMASK, (T + 0x155u) & TMASK, (T + 0x2AAu) & TMASK, (T + 0x0FFu) & TMASK}; }
__device__ __forceinline__ v4u pack4(f32x4 v, const Tag& t) {
  return v4u{(fbits(v.x) & VMASK) | t.t0, (fbits(v.y) & VMASK) | t.t1, (fbits(v.z) & VMASK) | t.t2, (fbits(v.w) & VMASK) | t.t3};
}
__device__ __forceinline__ v4u pack2x(unsigned a, unsigned b, const Tag& t) {
  return v4u{(a & VMASK) | t.t0, ((a & TMASK) << 10) | t.t1, (b & VMASK) | t.t2, ((b & TMASK) << 10) | t.t3};
}
__device__ __forceinline__ v4u untag(v4u g, const Tag& t) { return v4u{g.x ^ t.t0, g.y ^ t.t1, g.z ^ t.t2, g.w ^ t.t3}; }
__device__ __forceinline__ unsigned resid(v4u u) { return (u.x | u.y | u.z) | u.w; }
__device__ __forceinline__ f32x4 asf4(v4u u) { return __builtin_bit_cast(f32x4, u); }

template <int N>
__device__ __forceinline__ void poll(rsrc_t r, const unsigned (&off)[N], const bool (&live)[N], const Tag& tag, v4u (&res)[N]) {
  unsigned spins = 0;
#pragma nounroll
  for (;;) {
    v4u a[N];
#pragma unroll
    for (int i = 0; i < N; ++i) a[i] = ld_sc1(r, off[i]);
    CBAR();
    unsigned bad = 0;
#pragma unroll
    for (int i = 0; i < N; ++i) { res[i] = untag(a[i], tag); bad |= live[i] ? resid(res[i]) : 0u; }
    if (__all((bad & TMASK) == 0u) || ++spins > SPIN_CAP) break;
    __builtin_amdgcn_s_sleep(RETRY);
  }
}
__device__ __forceinline__ float final_sum(rsrc_t rws, unsigned base, int lane, const Tag& tag) {
  unsigned off[4]; bool live[4]; v4u g[4];
#pragma unroll
  for (int i = 0; i < 4; ++i) { off[i] = base + (unsigned)((i * 64 + lane) * 2304); live[i] = true; }
  poll<4>(rws, off, live, tag, g);
  const f32x4 v = (asf4(g[0]) + asf4(g[1])) + (asf4(g[2]) + asf4(g[3]));
  return x16_sum(tr_sum16(v, lane));
}

__device__ __forceinline__ void lds_wait(volatile LAS unsigned* f, unsigned want) {
  unsigned spins = 0;
#pragma nounroll
  while (*f < want) { if (++spins > (SPIN_CAP << 6)) break; }
  CBAR();
}
__device__ __forceinline__ void lds_wait_slow(volatile LAS unsigned* f, unsigned want) {
  unsigned spins = 0;
#pragma nounroll
  while (*f < want) { __builtin_amdgcn_s_sleep(2); if (++spins > (SPIN_CAP << 4)) break; }
  CBAR();
}
__device__ __forceinline__ void lds_add(volatile LAS unsigned* f) { __hip_atomic_fetch_add((LAS unsigned*)f, 1u, __ATOMIC_RELAXED, __HIP_MEMORY_SCOPE_WORKGROUP); }

struct Sel { unsigned m0, m1, m2, m3; };
__device__ __forceinline__ Sel mk_sel(int lane) {
  const unsigned h = (lane & 1) ? 0xFFFF0000u : 0x0000FFFFu; const int r = (lane & 7) >> 1;
  return Sel{r == 0 ? h : 0u, r == 1 ? h : 0u, r == 2 ? h : 0u, r == 3 ? h : 0u};
}
__device__ __forceinline__ v4u sel_b(unsigned dup, const Sel& s) { return v4u{dup & s.m0, dup & s.m1, dup & s.m2, dup & s.m3}; }
__device__ __forceinline__ f32x4 strip_fold(f32x4 d) {
  f32x4 y;
#pragma unroll
  for (int i = 0; i < 4; ++i) {
    const float t = dpp<ROR8>(d[i]);
    auto s = __builtin_amdgcn_permlane32_swap(fbits(t), fbits(t), false, false);
    y[i] = d[i] + asf(s[1]);
  }
  return y;
}

struct Ctx {
  LAS unsigned char* L; volatile LAS unsigned* flags; rsrc_t rws;
  int lane, g8, c8, b, xg, idx, kvh, sp;
};
__device__ __forceinline__ void wo_offsets(const Ctx& c, int st, int g8, int c8, unsigned (&off)[2]) {
  const int hrow = (2 * c.kvh + (c.sp >> 3)) * 128 + 32 * ((c.sp >> 1) & 3) + 4 * (c.sp & 1);
#pragma unroll
  for (int t = 0; t < 2; ++t) { const int u = 2 * g8 + t; off[t] = (unsigned)(((hrow + 8 * (u & 3) + (u >> 2)) * D + 64 * st + 8 * c8) * 2); }
}
__device__ __forceinline__ void wd_offsets(const Ctx& c, int st, int g8, int c8, unsigned (&off)[3]) {
  const int odd = c.idx & 1, g0 = 9 * (c.idx >> 1) + 4 * odd;
#pragma unroll
  for (int t = 0; t < 3; ++t) {
    const int u = t < 2 ? 2 * g8 + t : 16 + (g8 & 3), Gi = g0 + (u >> 2);
    const int row = 576 * c.xg + 64 * (Gi >> 4) + 32 * ((Gi >> 3) & 1) + 8 * (u & 3) + (Gi & 7);
    off[t] = (t < 2 || (odd && g8 < 4)) ? (unsigned)((row * D + 64 * st + 8 * c8) * 2) : OOB;
  }
}
__device__ __forceinline__ unsigned strip_off(int lane, int st) {
  unsigned o = (lane < 32 && (lane & 15) < 8) ? (unsigned)((st * 16 + (lane >> 4) * 8 + (lane & 7)) * 16) : 0x80000000u;
  asm volatile("" : "+v"(o));
  return o;
}
__device__ __forceinline__ void publish_at(const Ctx& c, unsigned off, f32x4 acc, const Tag& tag) {
  st_sc1(c.rws, off, pack4(strip_fold(acc), tag));
}
__device__ __forceinline__ void wo_partial(const Ctx& c, unsigned poff, v2u o2, const v4u (&WO)[2], const Sel& sel, const Tag& tag) {
  f32x4 acc = {0.f, 0.f, 0.f, 0.f};
  acc = mfma(WO[0], sel_b(o2.x, sel), acc);
  acc = mfma(WO[1], sel_b(o2.y, sel), acc);
  publish_at(c, poff + WS_SP + c.b * 2304, acc, tag);
  if (c.lane == 0) lds_add(&c.flags[C_SP]);
  PIN();
}
__device__ __forceinline__ void wd_partial(const Ctx& c, unsigned poff, v2u h2, unsigned h3, const v4u (&WD)[3], const Sel& sel, const Tag& tag) {
  f32x4 acc = {0.f, 0.f, 0.f, 0.f};
  acc = mfma(WD[0], sel_b(h2.x, sel), acc);
  acc = mfma(WD[1], sel_b(h2.y, sel), acc);
  acc = mfma(WD[2], sel_b(c.g8 < 4 ? h3 : 0u, sel), acc);
  publish_at(c, poff + WS_FP + c.b * 2304, acc, tag);
  if (c.lane == 0) lds_add(&c.flags[C_FP]);
  PIN();
}

#define STAMP(i) do {} while (0)
#define STAMP_INIT() do {} while (0)
#define STAMP_FLUSH() do {} while (0)

__global__ void __launch_bounds__(NTHR, 1) fused_decode(Params p) {
  extern __shared__ __attribute__((aligned(16))) unsigned char smem[];
  LAS unsigned char* L = (LAS unsigned char*)smem;
  volatile LAS unsigned* flags = (volatile LAS unsigned*)(L + L_FLAG);
  const int lane = threadIdx.x & 63;
  const int wave = UNI(threadIdx.x >> 6);
  const int b = blockIdx.x, xg = b & 7, idx = b >> 3;
  const int kvh = 2 * xg + (idx >> 4), sp = idx & 15;
  const rsrc_t rws = mk_rsrc(p.ws, WS_END);
  const int lg = (idx >> 3) * 8 + xg;
  const int rfin = lg * 8 + (idx & 7);
  const int g8 = lane >> 3, c8 = lane & 7;
  const Ctx c{L, flags, rws, lane, g8, c8, b, xg, idx, kvh, sp};

  unsigned long long code_base;
  asm volatile("s_getpc_b64 %0" : "=s"(code_base));
  code_base &= ~255ull;
  unsigned cpf0 = 0, cpf1 = 0, cpf2 = 0;
  if ((threadIdx.x >> 6) == 10 && (int)(blockIdx.x >> 3) < CODE_PF_WGS) {
    const GAS unsigned* cp = (const GAS unsigned*)code_base + (threadIdx.x & 63) * 32;
    if ((threadIdx.x & 63) * 128 + 16384 < CODE_BYTES) cpf2 = cp[4096];
    if ((threadIdx.x & 63) * 128 + 8192 < CODE_BYTES) cpf1 = cp[2048];
    cpf0 = cp[0];
  }
  PIN();
  STAMP_INIT();
  STAMP(0);
  const unsigned t_start = now32();
  asm volatile("" :: "s"(p.x), "s"(p.kc), "s"(p.vc), "s"(p.wq), "s"(p.wo), "s"(p.wg), "s"(p.wu), "s"(p.wd), "s"(p.out), "s"(p.ws));
  if (wave == 0 && lane < 52) flags[lane] = lane < 16 ? 0u : EMPTY;
  WAIT_LGKM0();
  __builtin_amdgcn_s_barrier();
  PIN();
  STAMP(12);

  if (wave == 10) {
    const unsigned E = __hip_atomic_load((const GAS unsigned*)(p.ws + WS_EPOCH), __ATOMIC_RELAXED, __HIP_MEMORY_SCOPE_AGENT);
    const rsrc_t rx = mk_rsrc(p.x, D * 2);
    __builtin_amdgcn_raw_ptr_buffer_load_lds(rx, (LAS void*)(L + L_X), 16, lane * 16, 0, 0, 0);
    __builtin_amdgcn_raw_ptr_buffer_load_lds(rx, (LAS void*)(L + L_X + 1024), 16, 1024 + lane * 16, 0, 0, 0);
    const rsrc_t rq = mk_rsrc(p.wq, D * NQ * 2);
    const unsigned qoff = (unsigned)(((144 * (sp >> 2) + 18 * g8) * NQ + kvh * 256 + 64 * (sp & 3) + 8 * c8) * 2);
#pragma unroll
    for (int t = 0; t < 18; ++t) __builtin_amdgcn_raw_ptr_buffer_load_lds(rq, (LAS void*)(L + L_WQ + 1024 * t), 16, qoff + t * (NQ * 2), 0, 0, AUX_NT);
    PIN();
    STAMP(13);
    __builtin_amdgcn_s_waitcnt(0x4F72);
    const unsigned T = UNI(E) + 1u;
    asm volatile("ds_write_b32 %0, %1 offset:%2\n\ts_waitcnt lgkmcnt(0)\n\tds_write_b32 %0, %3 offset:%4" :: "v"(0u), "v"(T), "i"(L_FLAG + 4 * W_TAG), "v"(1u), "i"(L_FLAG + 4 * F_X) : "memory");
    st_sc1(rws, (idx == 0 && lane == 0) ? (unsigned)(WS_TB + xg * 128) : OOB, pack2x(t_start, 0u, mk_tag(T)));
    __builtin_amdgcn_s_waitcnt(0x0F71);
    asm volatile("ds_write_b32 %0, %1 offset:%2" :: "v"(0u), "v"(1u), "i"(L_FLAG + 4 * F_WQ) : "memory");
    STAMP(14);
  }
  unsigned wave2 = wave;
  asm volatile("" : "+s"(wave2));

  if (wave2 - 1u >= 9u) {
  if (wave2 == 0) {
    const int st = sp & 3, rs = sp >> 2;
    lds_wait(&flags[F_X], 1u);
    const LAS unsigned* xl = (const LAS unsigned*)(L + L_X);
    const LAS u16* xh = (const LAS u16*)(L + L_X);
    unsigned xw[5];
#pragma unroll
    for (int i = 0; i < 4; ++i) xw[i] = xl[lane + 64 * i];
    xw[4] = lane < 32 ? xl[256 + lane] : 0u;
    const unsigned xq0 = xh[144 * rs + lane], xq1 = xh[144 * rs + 64 + lane], xq2 = xh[144 * rs + 128 + (lane & 15)];
    const unsigned xg0 = xh[36 * sp + (lane < 36 ? lane : 0)];
    const unsigned xr0 = xh[64 * (rfin >> 4) + 32 * ((rfin >> 3) & 1) + 8 * (lane & 3) + (rfin & 7)];
    const Tag tag = mk_tag(flags[W_TAG]);
    float xv[10];
#pragma unroll
    for (int i = 0; i < 5; ++i) { xv[2 * i] = asf(xw[i] << 16); xv[2 * i + 1] = asf(xw[i] & 0xffff0000u); }
    const float s = ((xv[0] + xv[1]) + (xv[2] + xv[3])) + ((xv[4] + xv[5]) + (xv[6] + xv[7])) + (xv[8] + xv[9]);
    const float mean = wave_sum_s(s) * (1.f / D);
    float q2 = 0.f, q3 = 0.f;
#pragma unroll
    for (int i = 0; i < 8; i += 2) { const float d0 = xv[i] - mean, d1 = xv[i + 1] - mean; q2 = fmaf(d0, d0, q2); q3 = fmaf(d1, d1, q3); }
    { const float d0 = lane < 32 ? xv[8] - mean : 0.f, d1 = lane < 32 ? xv[9] - mean : 0.f; q2 = fmaf(d0, d0, q2); q3 = fmaf(d1, d1, q3); }
    const float var = wave_sum_s(q2 + q3) * (1.f / D);
    const float mb = rbf(mean);
    const float den = rbf(__builtin_amdgcn_sqrtf(rbf(rbf(var) + 1e-5f)));
    const float rden = __builtin_amdgcn_rcpf(den);
    auto norm = [&](unsigned raw) -> unsigned {
      const float a = rbf(asf(raw << 16) - mb);
      float qv = a * rden; qv = fmaf(fmaf(-qv, den, a), rden, qv);
      return cvtpk(qv, qv);
    };
    LAS unsigned* xq = (LAS unsigned*)(L + L_XQ);
    xq[lane] = norm(xq0); xq[64 + lane] = norm(xq1);
    { const unsigned v2 = norm(xq2); if (lane < 16) xq[128 + lane] = v2; }
    WAIT_LGKM0();
    const Sel sel = mk_sel(lane);
    unsigned xv18[18];
    {
      const LAS unsigned* src = xq + 18 * g8;
#pragma unroll
      for (int i = 0; i < 9; ++i) { const v2u t2 = *(const LAS v2u*)(src + 2 * i); xv18[2 * i] = t2.x; xv18[2 * i + 1] = t2.y; }
    }
    STAMP(1);
    f32x4 acc = {0.f, 0.f, 0.f, 0.f};
    lds_wait(&flags[F_WQ], 1u);
#pragma unroll
    for (int t = 0; t < 18; ++t) acc = mfma(((const LAS v4u*)(L + L_WQ))[t * 64 + lane], sel_b(xv18[t], sel), acc);
    {
      const f32x4 y = strip_fold(acc);
      if (lane < 32 && (lane & 15) < 8)
        st_sc1(rws, (unsigned)(WS_QP + ((kvh * 4 + rs) * 4 + st) * 256 + ((lane >> 4) * 8 + c8) * 16), pack4(y, tag));
    }
    STAMP(2);
    { const unsigned vg = norm(xg0); if (lane < 40) ((LAS unsigned*)(L + L_XG))[lane] = lane < 36 ? vg : 0u; }
    if (lane < 4) ((LAS float*)(L + L_XR))[lane] = asf(xr0 << 16);
    WAIT_LGKM0();
    if (lane == 0) flags[F_XN] = 1u;
    {
      unsigned off[4]; bool live[4]; v4u g[4];
#pragma unroll
      for (int r = 0; r < 4; ++r) { off[r] = (unsigned)(WS_QP + (kvh * 4 + r) * 1024 + lane * 16); live[r] = true; }
      __builtin_amdgcn_s_sleep(DLY_Q);
      poll<4>(rws, off, live, tag, g);
      const f32x4 q4 = (asf4(g[0]) + asf4(g[1])) + (asf4(g[2]) + asf4(g[3]));
      LAS u16* qb = (LAS u16*)(L + L_QB) + 64 * (lane >> 4) + 32 * (g8 & 1) + c8;
      const unsigned p01 = cvtpk(q4[0], q4[1]), p23 = cvtpk(q4[2], q4[3]);
      qb[0] = (u16)p01; qb[8] = (u16)(p01 >> 16); qb[16] = (u16)p23; qb[24] = (u16)(p23 >> 16);
      WAIT_LGKM0();
      if (lane == 0) flags[F_Q] = 1u;
    }
    STAMP(3);
    unsigned ooff[2];
    wo_offsets(c, 8, g8, c8, ooff);
    const unsigned poff8 = strip_off(lane, 8);
    const rsrc_t ro = mk_rsrc(p.wo, NQ * D * 2);
    lds_wait_slow(&flags[F_OP], 1u);
    STAMP(4);
    v4u WO[2];
    WO[0] = ld_nt(ro, ooff[0]); WO[1] = ld_nt(ro, ooff[1]);
    PIN();
    {
      const unsigned pb = (unsigned)(WS_OP + (kvh * 16 + (lane & 15)) * 1152);
      unsigned off[2] = {pb + (4 * sp + (lane >> 4)) * 16, pb + (64 + (sp >> 3)) * 16};
      bool live[2] = {true, true}; v4u g[2];
      __builtin_amdgcn_s_sleep(DLY_O);
      poll<2>(rws, off, live, tag, g);
      const float m = asf(g[1].x | (g[1].y >> 10)), l = asf(g[1].z | (g[1].w >> 10));
      const float M = row_max16(m);
      const float wgt = __builtin_amdgcn_exp2f(m - M);
      const float Ls = row_sum16(wgt * l);
      const float o = tr_sum16(wgt * asf4(g[0]), lane) * __builtin_amdgcn_rcpf(Ls);
      if ((lane & 3) == 0) ((LAS unsigned*)(L + L_OS))[lane >> 2] = cvtpk(o, o);
      WAIT_LGKM0();
    }
    STAMP(5);
    wo_partial(c, poff8, *(const LAS v2u*)(L + L_OS + 8 * g8), WO, sel, tag);
    if (lg < 18) {
      lds_wait_slow(&flags[C_SP], 9u);
      __builtin_amdgcn_s_sleep(DLY_S);
      const float sv = final_sum(rws, (unsigned)(WS_SP + rfin * 16), lane, tag);
      if (lane < 16 && (lane & 3) == 0) ((LAS float*)(L + L_FIN))[lane >> 2] = sv;
      WAIT_LGKM0();
      if (lane == 0) flags[F_S] = 1u;
      STAMP(6);
    }
  } else {
    const Tag tag = mk_tag(flags[W_TAG]);
    int late;
    {
      wait_until(t_start + TB_READ);
      const v4u g = untag(ld_sc1(rws, lane < 8 ? (unsigned)(WS_TB + lane * 128) : OOB), tag);
      const bool ok = lane < 8 && (resid(g) & TMASK) == 0u;
      late = ok ? (int)(t_start - (g.x | (g.y >> 10))) : 0;
      late = late > TB_MAX ? 0 : late;
      late = max(late, __builtin_amdgcn_mov_dpp(late, XOR1, 0xf, 0xf, true));
      late = max(late, __builtin_amdgcn_mov_dpp(late, XOR2, 0xf, 0xf, true));
      late = max(late, __builtin_amdgcn_mov_dpp(late, XOR7, 0xf, 0xf, true));
      late = UNI(late);
      if (lane == 0 && late > 0) flags[W_SHIFT] = (unsigned)late;
    }
    asm volatile("" :: "v"(cpf0), "v"(cpf1), "v"(cpf2));
    const Sel sel = mk_sel(lane);
    const int g0 = 9 * (idx >> 1) + 4 * (idx & 1), odd = idx & 1;
    unsigned doff[3];
    wd_offsets(c, 8, g8, c8, doff);
    const unsigned poff8 = strip_off(lane, 8);
    const rsrc_t rd = mk_rsrc(p.wd, F * D * 2);
    const bool lc = odd && lane < 32;
    const unsigned pb = (unsigned)(WS_GP + (xg * 32 + (lane & 15)) * 2304);
    unsigned off[3] = {pb + (g0 + (lane >> 4)) * 16, pb + 16 * 2304 + (g0 + (lane >> 4)) * 16, lc ? pb + (lane >> 4) * (16 * 2304) + (g0 + 4) * 16 : OOB};
    bool live[3] = {true, true, lc}; v4u g[3];
    const int vi = (lane >> 2) & 3;
    const int col = 64 * (rfin >> 4) + 32 * ((rfin >> 3) & 1) + 8 * vi + (rfin & 7);
    wait_until(t_start + T_LAST - (unsigned)late);
    v4u WD[3];
#pragma unroll
    for (int t = 0; t < 3; ++t) WD[t] = ld_nt(rd, doff[t]);
    PIN();
    lds_wait(&flags[C_GU], 9u);
    STAMP(1);
    __builtin_amdgcn_s_sleep(DLY_H);
    poll<3>(rws, off, live, tag, g);
    STAMP(2);
    if (!lc) g[2] = v4u{0u, 0u, 0u, 0u};
    {
      const float gs = tr_sum16(asf4(g[0]), lane), us = tr_sum16(asf4(g[1]), lane), cs = tr_sum16(asf4(g[2]), lane);
      auto sw = __builtin_amdgcn_permlane16_swap(fbits(cs), fbits(cs), false, false);
      const bool ex = (lane & 3) == 1;
      const float gin = rbf(ex ? asf(sw[0]) : gs), uin = rbf(ex ? asf(sw[1]) : us);
      const float hv = rbf(silu_f(gin)) * uin;
      LAS unsigned* hs = (LAS unsigned*)(L + L_HS);
      if ((lane & 3) == 0 || (ex && lane < 16)) hs[ex ? 16 + vi : lane >> 2] = cvtpk(hv, hv);
    }
    WAIT_LGKM0();
    STAMP(3);
    wd_partial(c, poff8, *(const LAS v2u*)(L + L_HS + 8 * g8), ((const LAS unsigned*)(L + L_HS))[16 + (g8 & 3)], WD, sel, tag);
    if (lg < 18) {
      const float xr = ((const LAS float*)(L + L_XR))[vi];
      lds_wait(&flags[C_FP], 9u);
      STAMP(4);
      __builtin_amdgcn_s_sleep(DLY_F);
      const float fv = final_sum(rws, (unsigned)(WS_FP + rfin * 16), lane, tag);
      STAMP(5);
      const float gate = rbf(silu_f(rbf(fv)));
      lds_wait(&flags[F_S], 1u);
      const float sv = ((const LAS float*)(L + L_FIN))[vi];
      const float res = xr + rbf(rbf(sv) * gate);
      if (lane < 16 && (lane & 3) == 0) p.out[col] = (u16)cvtpk(res, res);
      if (b == 0 && lane == 0) __hip_atomic_store((GAS unsigned*)(p.ws + WS_EPOCH), flags[W_TAG], __ATOMIC_RELAXED, __HIP_MEMORY_SCOPE_AGENT);
      STAMP(6);
    }
  }
  } else {
  const int st = wave - 1;
  const Sel sel = mk_sel(lane);
  const rsrc_t rg = mk_rsrc((idx >> 4) ? p.wu : p.wg, D * F * 2);
  unsigned goff, goff4, ooff[2], doff[3];
  auto late_addr = [&]() {
    PIN();
    int ln = lane; asm volatile("" : "+v"(ln));
    const int g8_ = ln >> 3, c8_ = ln & 7;
    const unsigned gcol = (unsigned)(576 * xg + 64 * st + 8 * c8_);
    goff = (unsigned)(((36 * sp + 4 * g8_) * F + gcol) * 2);
    goff4 = g8_ < 4 ? (unsigned)(((36 * sp + 32 + g8_) * F + gcol) * 2) : OOB;
    wo_offsets(c, st, g8_, c8_, ooff);
    wd_offsets(c, st, g8_, c8_, doff);
    PIN();
  };
  const unsigned poff = strip_off(lane, st);
  v4u G[5];
  unsigned tn;
#define SLOT(dt) do { wait_until(tn); tn += (dt); } while (0)
  auto issue_g = [&](int t) { SLOT(DT2); G[t] = ld_nt(rg, t < 4 ? goff + t * (F * 2) : goff4); PIN(); };
  v4u gb[5];
  auto gu_frags = [&]() {
    const v4u x4 = *(const LAS v4u*)(L + L_XG + 16 * g8);
    const unsigned x5 = ((const LAS unsigned*)(L + L_XG))[32 + g8];
#pragma unroll
    for (int t = 0; t < 4; ++t) gb[t] = sel_b(x4[t], sel);
    gb[4] = sel_b(x5, sel);
    PIN();
  };
  auto do_gu = [&](const Tag& tag) {
    f32x4 acc = {0.f, 0.f, 0.f, 0.f};
#pragma unroll
    for (int t = 0; t < 5; ++t) acc = mfma(G[t], gb[t], acc);
    publish_at(c, poff + WS_GP + (xg * 32 + idx) * 2304, acc, tag);
    if (lane == 0) lds_add(&flags[C_GU]);
    PIN();
  };

  if (wave > 8) {
    tn = now32() + TSTART + 2 * DT1;
    late_addr();
#pragma unroll
    for (int t = 0; t < 5; ++t) { issue_g(t); tn += W9GAP - DT2; }
    lds_wait_slow(&flags[F_XN], 1u);
    const Tag tag = mk_tag(flags[W_TAG]);
    gu_frags();
    do_gu(tag);
    STAMP(2);
    lds_wait(&flags[C_ATT], 8u);
    STAMP(3);
    const int hh = g8 & 1;
    f32x2 ml[8]; f32x4 po[8];
#pragma unroll
    for (int w = 0; w < 8; ++w) { ml[w] = ((const LAS f32x2*)(L + L_ML))[w * 2 + hh]; po[w] = ((const LAS f32x4*)(L + L_PO))[w * 64 + lane]; }
    const float M = fmaxf(fmaxf(fmaxf(ml[0].x, ml[1].x), fmaxf(ml[2].x, ml[3].x)), fmaxf(fmaxf(ml[4].x, ml[5].x), fmaxf(ml[6].x, ml[7].x)));
    f32x4 o = {0.f, 0.f, 0.f, 0.f}; float Ls = 0.f;
#pragma unroll
    for (int w = 0; w < 8; ++w) {
      const float sc = __builtin_amdgcn_exp2f(ml[w].x - M);
      Ls = fmaf(sc, ml[w].y, Ls);
      o += sc * po[w];
    }
    const unsigned pbase = (unsigned)(WS_OP + (kvh * 16 + sp) * 1152);
    st_sc1(rws, pbase + (hh * 32 + (lane >> 4) * 8 + c8) * 16, pack4(o, tag));
    if ((lane & 0x37) == 0) st_sc1(rws, pbase + (64 + hh) * 16, pack2x(fbits(M), fbits(Ls), tag));
    if (lane == 0) flags[F_OP] = 1u;
    PIN();
    STAMP(4);
  } else {
    const size_t tile = ((size_t)kvh * SK + (SK - WIN)) * DH;
    const rsrc_t rk = mk_rsrc(p.kc + tile, WIN * DH * 2);
    const rsrc_t rv = mk_rsrc(p.vc + tile, WIN * DH * 2);
    const int cbase = (st * 8 * 16 + sp) * 1024;
    const unsigned koff = (unsigned)(((lane >> 2) & 3) * 256 + ((lane & 3) + 4 * (lane >> 4)) * 16);
    STAMP(13);
    tn = now32() + TSTART + (st * DT1) / 8;
    v4u K[8], V[8];
#define LDK(t) do { SLOT(DT1); if (t >= KWIN) WAIT_VM(KWIN - 1); K[t] = __builtin_amdgcn_raw_buffer_load_b128(rk, (int)koff, cbase + t * 16384, AUX_NT); PIN(); } while (0)
#define LDV(t) do { SLOT(DT1); WAIT_VM(KWIN - 1); V[t] = __builtin_amdgcn_raw_buffer_load_b128(rv, lane * 16, cbase + t * 16384, AUX_NT); PIN(); } while (0)
    unsigned shifted = 0;
#define CATCH_UP() do { const unsigned sh_ = UNI(flags[W_SHIFT]); tn -= sh_ - shifted; shifted = sh_; } while (0)
#pragma unroll
    for (int t = 0; t < 8; ++t) { LDK(t); if (t == 1) late_addr(); if (t == 5) CATCH_UP(); }
    constexpr float C2 = 0.08838834764831845f * 1.4426950408889634f;
    unsigned pk[8];
    auto qk_softmax = [&]() {
      const v4u qf = *(const LAS v4u*)(L + L_QB + ((g8 & 1) * 128 + ((lane & 3) + 4 * (lane >> 4)) * 8) * 2);
      float s[8];
#pragma unroll
      for (int t = 0; t < 8; ++t) {
        const f32x4 d = mfma(K[t], qf, f32x4{0.f, 0.f, 0.f, 0.f});
        s[t] = (dpp<0x00>(d[0]) + dpp<0x55>(d[1])) + (dpp<0xAA>(d[2]) + dpp<0xFF>(d[3]));
      }
      float m = fmaxf(fmaxf(fmaxf(s[0], s[1]), fmaxf(s[2], s[3])), fmaxf(fmaxf(s[4], s[5]), fmaxf(s[6], s[7])));
      m = x16_max(m) * C2;
      float l = 0.f;
#pragma unroll
      for (int t = 0; t < 8; ++t) { const float pr = __builtin_amdgcn_exp2f(fmaf(s[t], C2, -m)); l += pr; pk[t] = cvtpk(pr, pr); }
      l = x16_sum(l);
      if ((lane & 0x37) == 0) ((LAS f32x2*)(L + L_ML))[st * 2 + g8] = f32x2{m, l};
      PIN();
    };
#pragma unroll
    for (int t = 0; t < QKPOS; ++t) { LDV(t); if (t == 0 || t == 3) CATCH_UP(); }
    const bool early = UNI(flags[F_Q]) != 0u;
    if (early) qk_softmax();
    STAMP(3);
#pragma unroll
    for (int t = QKPOS; t < 8; ++t) LDV(t);
    STAMP(11);
    tn -= (st * DT1) / 8;
#pragma unroll
    for (int t = 0; t < QKG; ++t) issue_g(t);
    STAMP(1);
    if (!early) { lds_wait(&flags[F_Q], 1u); qk_softmax(); }
    STAMP(2);
    f32x4 oacc = {0.f, 0.f, 0.f, 0.f};
#pragma unroll
    for (int t = 0; t < 8; ++t) oacc = mfma(V[t], sel_b(pk[t], sel), oacc);
    ((LAS f32x4*)(L + L_PO))[st * 64 + lane] = oacc;
    WAIT_LGKM0();
    if (lane == 0) lds_add(&flags[C_ATT]);
    PIN();
    STAMP(4);
#pragma unroll
    for (int t = QKG; t < 5; ++t) issue_g(t);
    const Tag tag = mk_tag(flags[W_TAG]);
    gu_frags();
    const rsrc_t ro = mk_rsrc(p.wo, NQ * D * 2);
    const rsrc_t rd = mk_rsrc(p.wd, F * D * 2);
    v4u WO[2], WD[3];
    f32x4 gacc = {0.f, 0.f, 0.f, 0.f};
#pragma unroll
    for (int t = 0; t < 3; ++t) gacc = mfma(G[t], gb[t], gacc);
    PIN();
    tn += DTD - DT2;
    SLOT(DTD); WD[0] = ld_nt(rd, doff[0]); PIN();
    STAMP(5);
    gacc = mfma(G[3], gb[3], gacc);
    gacc = mfma(G[4], gb[4], gacc);
    publish_at(c, poff + WS_GP + (xg * 32 + idx) * 2304, gacc, tag);
    if (lane == 0) lds_add(&flags[C_GU]);
    PIN();
    STAMP(6);
    SLOT(DTD); WD[1] = ld_nt(rd, doff[1]); PIN();
    if (idx & 1) SLOT(DTD);
    WD[2] = ld_nt(rd, doff[2]); PIN();
    tn += DTO - DTD;
    SLOT(DT2); WO[0] = ld_nt(ro, ooff[0]); PIN();
    SLOT(DT2); WO[1] = ld_nt(ro, ooff[1]); PIN();
    v2u h2, o2; unsigned h3, spins = 0; bool hh;
#pragma nounroll
    for (;;) {
      h2 = *(const volatile LAS v2u*)(L + L_HS + 8 * g8); h3 = ((const volatile LAS unsigned*)(L + L_HS))[16 + (g8 & 3)];
      o2 = *(const volatile LAS v2u*)(L + L_OS + 8 * g8);
      hh = __all(h2.x != EMPTY && h2.y != EMPTY && h3 != EMPTY);
      if (hh || __all(o2.x != EMPTY && o2.y != EMPTY) || ++spins > (SPIN_CAP << 6)) break;
    }
    if (hh) {
      wd_partial(c, poff, h2, h3, WD, sel, tag);
      STAMP(8);
#pragma nounroll
      while (!__all(o2.x != EMPTY && o2.y != EMPTY) && ++spins <= (SPIN_CAP << 6)) o2 = *(const volatile LAS v2u*)(L + L_OS + 8 * g8);
      wo_partial(c, poff, o2, WO, sel, tag);
      STAMP(7);
    } else {
      wo_partial(c, poff, o2, WO, sel, tag);
      STAMP(7);
#pragma nounroll
      while (!__all(h2.x != EMPTY && h2.y != EMPTY && h3 != EMPTY) && ++spins <= (SPIN_CAP << 6)) {
        h2 = *(const volatile LAS v2u*)(L + L_HS + 8 * g8); h3 = ((const volatile LAS unsigned*)(L + L_HS))[16 + (g8 & 3)];
      }
      wd_partial(c, poff, h2, h3, WD, sel, tag);
      STAMP(8);
    }
  }
  }
  STAMP_FLUSH();
}

}

extern "C" void kernel_launch(void* const* d_in, const int* in_sizes, int n_in, void* d_out, int out_size, void* d_ws, size_t ws_size, hipStream_t stream) {
  Params p;
  p.x = (const u16*)d_in[0]; p.kc = (const u16*)d_in[1]; p.vc = (const u16*)d_in[2];
  p.wq = (const u16*)d_in[3]; p.wo = (const u16*)d_in[4];
  p.wg = (const u16*)d_in[6]; p.wu = (const u16*)d_in[7]; p.wd = (const u16*)d_in[8];
  p.out = (u16*)d_out; p.ws = (unsigned char*)d_ws;
  (void)hipFuncSetAttribute((const void*)fused_decode, hipFuncAttributeMaxDynamicSharedMemorySize, LDS_BYTES);
  hipLaunchKernelGGL(fused_decode, dim3(NWG), dim3(NTHR), LDS_BYTES, stream, p);
}
```

```cpp
#include <hip/hip_runtime.h>
#include <hip/hip_bf16.h>
#include <stdint.h>

#define GAS __attribute__((address_space(1)))
#define LAS __attribute__((address_space(3)))
typedef unsigned v4u __attribute__((ext_vector_type(4)));
typedef unsigned v2u __attribute__((ext_vector_type(2)));
typedef float f32x4 __attribute__((ext_vector_type(4)));
typedef float f32x2 __attribute__((ext_vector_type(2)));
typedef short bf16x8 __attribute__((ext_vector_type(8)));
typedef __bf16 bf16x2 __attribute__((ext_vector_type(2)));
typedef unsigned short u16;
typedef __amdgpu_buffer_rsrc_t rsrc_t;

#define KWIN 4
#define DT1 34
#define DT2 36
#define DTD 36
#define DTO 36
#define DRY0 1
#define TLBP 7
#define TSTART 10
#define QKPOS 6
#define QKG 3
#define W9GAP 70
#define T_LAST 790
#define CODE_BYTES 17920
#define CODE_PF_WGS 2
#define TB_READ 170
#define TB_MAX 300
#define DLY_Q 36
#define DLY_O 30
#define DLY_H 20
#define DLY_F 20
#define DLY_S 28
#define RETRY 8

namespace {

constexpr int D = 576, SK = 65536, DH = 128, WIN = 4096, F = 4608, NQ = 4096;
constexpr int NWG = 256, NWAVE = 11, NTHR = NWAVE * 64;
constexpr int LDS_BYTES = 84 * 1024;

constexpr int WS_EPOCH = 0;
constexpr int WS_TB = 256;
constexpr int WS_QP = WS_TB + 1024;
constexpr int WS_OP = WS_QP + 16 * 4096;
constexpr int WS_GP = WS_OP + 16 * 16 * 1152;
constexpr int WS_SP = WS_GP + 8 * 32 * 2304;
constexpr int WS_FP = WS_SP + 256 * 2304;
constexpr int WS_END = WS_FP + 256 * 2304;

constexpr int L_FLAG = 0;
constexpr int L_OS = 64;
constexpr int L_HS = L_OS + 64;
constexpr int L_XQ = L_HS + 128;
constexpr int L_XG = L_XQ + 576;
constexpr int L_XR = L_XG + 160;
constexpr int L_QB = L_XR + 16;
constexpr int L_ML = L_QB + 512;
constexpr int L_FIN = L_ML + 128;
constexpr int L_PO = L_FIN + 16;
constexpr int L_X = L_PO + 8192;
constexpr int L_WQ = L_X + 2048;
constexpr int L_TS = L_WQ + 18 * 1024;
static_assert(L_PO % 16 == 0 && L_XQ % 16 == 0 && L_XG % 16 == 0 && L_QB % 16 == 0 && L_OS % 16 == 0 && L_HS % 16 == 0 && L_XR % 16 == 0 && L_X % 16 == 0, "align");
enum { F_XN = 0, F_Q = 1, C_ATT = 2, C_GU = 3, F_OP = 4, C_SP = 5, C_FP = 6, F_S = 7, W_TAG = 8, W_SHIFT = 9, F_X = 10, F_WQ = 11 };

constexpr int AUX_NT = 2, AUX_SC1 = 16;
constexpr unsigned VMASK = 0xFFFFFC00u, TMASK = 0x3FFu, OOB = 0xFFFFFF00u;
constexpr unsigned EMPTY = 0xFFFF0000u;
constexpr unsigned SPIN_CAP = 1u << 17;

struct Params {
  const u16* x; const u16* kc; const u16* vc; const u16* wq; const u16* wo;
  const u16* wg; const u16* wu; const u16* wd; u16* out; unsigned char* ws;
};

__device__ __forceinline__ unsigned fbits(float f) { return __builtin_bit_cast(unsigned, f); }
__device__ __forceinline__ float asf(unsigned u) { return __builtin_bit_cast(float, u); }
__device__ __forceinline__ unsigned cvtpk(float a, float b) { bf16x2 r = {(__bf16)a, (__bf16)b}; return __builtin_bit_cast(unsigned, r); }
__device__ __forceinline__ float rbf(float f) { return asf(cvtpk(0.f, f) & 0xffff0000u); }
__device__ __forceinline__ float silu_f(float x) { return x * __builtin_amdgcn_rcpf(1.f + __builtin_amdgcn_exp2f(x * -1.4426950408889634f)); }
__device__ __forceinline__ f32x4 mfma(v4u a, v4u b, f32x4 c) {
  return __builtin_amdgcn_mfma_f32_16x16x32_bf16(__builtin_bit_cast(bf16x8, a), __builtin_bit_cast(bf16x8, b), c, 0, 0, 0);
}
template <int CTRL> __device__ __forceinline__ float dpp(float x) {
  return asf(__builtin_amdgcn_mov_dpp(__builtin_bit_cast(int, x), CTRL, 0xf, 0xf, true));
}
template <int CTRL, int RM> __device__ __forceinline__ float dppm(float x) {
  return asf(__builtin_amdgcn_update_dpp(0, __builtin_bit_cast(int, x), CTRL, RM, 0xf, true));
}
constexpr int XOR1 = 0xB1, XOR2 = 0x4E, XOR7 = 0x141, ROR8 = 0x128;
__device__ __forceinline__ float x16_max(float x) {
  auto s = __builtin_amdgcn_permlane16_swap(fbits(x), fbits(x), false, false);
  x = fmaxf(asf(s[0]), asf(s[1]));
  auto t = __builtin_amdgcn_permlane32_swap(fbits(x), fbits(x), false, false);
  return fmaxf(asf(t[0]), asf(t[1]));
}
__device__ __forceinline__ float x16_sum(float x) {
  auto s = __builtin_amdgcn_permlane16_swap(fbits(x), fbits(x), false, false);
  x = asf(s[0]) + asf(s[1]);
  auto t = __builtin_amdgcn_permlane32_swap(fbits(x), fbits(x), false, false);
  return asf(t[0]) + asf(t[1]);
}
__device__ __forceinline__ float row_sum16(float x) { x += dpp<XOR1>(x); x += dpp<XOR2>(x); x += dpp<XOR7>(x); x += dpp<ROR8>(x); return x; }
__device__ __forceinline__ float row_max16(float x) { x = fmaxf(x, dpp<XOR1>(x)); x = fmaxf(x, dpp<XOR2>(x)); x = fmaxf(x, dpp<XOR7>(x)); x = fmaxf(x, dpp<ROR8>(x)); return x; }
__device__ __forceinline__ float wave_sum_s(float s) {
  s += dpp<0x111>(s); s += dpp<0x112>(s); s += dpp<0x114>(s); s += dpp<0x118>(s);
  s += dppm<0x142, 0xa>(s); s += dppm<0x143, 0xc>(s);
  return asf(__builtin_amdgcn_readlane(fbits(s), 63));
}
__device__ __forceinline__ float tr_sum16(f32x4 v, int lane) {
  const bool b3 = lane & 8, b2 = lane & 4;
  const float a = (b3 ? v[2] : v[0]) + dpp<ROR8>(b3 ? v[0] : v[2]);
  const float b = (b3 ? v[3] : v[1]) + dpp<ROR8>(b3 ? v[1] : v[3]);
  float r = (b2 ? b : a) + dpp<XOR7>(b2 ? a : b);
  r += dpp<XOR2>(r); r += dpp<XOR1>(r);
  return r;
}

__device__ __forceinline__ rsrc_t mk_rsrc(const void* p, unsigned bytes) {
  return __builtin_amdgcn_make_buffer_rsrc((void*)p, 0, (int)bytes, 0x00020000);
}
__device__ __forceinline__ v4u ld_nt(rsrc_t r, unsigned off) { return __builtin_amdgcn_raw_buffer_load_b128(r, (int)off, 0, AUX_NT); }
__device__ __forceinline__ v4u ld_sc1(rsrc_t r, unsigned off) { return __builtin_amdgcn_raw_buffer_load_b128(r, (int)off, 0, AUX_SC1); }
__device__ __forceinline__ void st_sc1(rsrc_t r, unsigned off, v4u g) { __builtin_amdgcn_raw_buffer_store_b128(g, r, (int)off, 0, AUX_SC1); }
__device__ __forceinline__ unsigned now32() { unsigned long long t; asm volatile("s_memrealtime %0\n\ts_waitcnt lgkmcnt(0)" : "=s"(t) :: "memory"); return (unsigned)t; }
__device__ __forceinline__ void wait_until(unsigned t) {
#pragma nounroll
  while ((int)(now32() - t) < 0) __builtin_amdgcn_s_sleep(1);
}
#define PIN() __builtin_amdgcn_sched_barrier(0)
#define CBAR() asm volatile("" ::: "memory")
#define WAIT_VM(n) asm volatile("s_waitcnt vmcnt(%0)" ::"i"(n) : "memory")
#define WAIT_LGKM0() asm volatile("s_waitcnt lgkmcnt(0)" ::: "memory")
#define UNI(x) __builtin_amdgcn_readfirstlane(x)

struct Tag { unsigned t0, t1, t2, t3; };
__device__ __forceinline__ Tag mk_tag(unsigned T) { return Tag{T & TMASK, (T + 0x155u) & TMASK, (T + 0x2AAu) & TMASK, (T + 0x0FFu) & TMASK}; }
__device__ __forceinline__ v4u pack4(f32x4 v, const Tag& t) {
  return v4u{(fbits(v.x) & VMASK) | t.t0, (fbits(v.y) & VMASK) | t.t1, (fbits(v.z) & VMASK) | t.t2, (fbits(v.w) & VMASK) | t.t3};
}
__device__ __forceinline__ v4u pack2x(unsigned a, unsigned b, const Tag& t) {
  return v4u{(a & VMASK) | t.t0, ((a & TMASK) << 10) | t.t1, (b & VMASK) | t.t2, ((b & TMASK) << 10) | t.t3};
}
__device__ __forceinline__ v4u untag(v4u g, const Tag& t) { return v4u{g.x ^ t.t0, g.y ^ t.t1, g.z ^ t.t2, g.w ^ t.t3}; }
__device__ __forceinline__ unsigned resid(v4u u) { return (u.x | u.y | u.z) | u.w; }
__device__ __forceinline__ f32x4 asf4(v4u u) { return __builtin_bit_cast(f32x4, u); }

template <int N>
__device__ __forceinline__ void poll(rsrc_t r, const unsigned (&off)[N], const bool (&live)[N], const Tag& tag, v4u (&res)[N]) {
  unsigned spins = 0;
#pragma nounroll
  for (;;) {
    v4u a[N];
#pragma unroll
    for (int i = 0; i < N; ++i) a[i] = ld_sc1(r, off[i]);
    CBAR();
    unsigned bad = 0;
#pragma unroll
    for (int i = 0; i < N; ++i) { res[i] = untag(a[i], tag); bad |= live[i] ? resid(res[i]) : 0u; }
    if (__all((bad & TMASK) == 0u) || ++spins > SPIN_CAP) break;
    __builtin_amdgcn_s_sleep(RETRY);
  }
}
__device__ __forceinline__ float final_sum(rsrc_t rws, unsigned base, int lane, const Tag& tag) {
  unsigned off[4]; bool live[4]; v4u g[4];
#pragma unroll
  for (int i = 0; i < 4; ++i) { off[i] = base + (unsigned)((i * 64 + lane) * 2304); live[i] = true; }
  poll<4>(rws, off, live, tag, g);
  const f32x4 v = (asf4(g[0]) + asf4(g[1])) + (asf4(g[2]) + asf4(g[3]));
  return x16_sum(tr_sum16(v, lane));
}

__device__ __forceinline__ void lds_wait(volatile LAS unsigned* f, unsigned want) {
  unsigned spins = 0;
#pragma nounroll
  while (*f < want) { if (++spins > (SPIN_CAP << 6)) break; }
  CBAR();
}
__device__ __forceinline__ void lds_wait_slow(volatile LAS unsigned* f, unsigned want) {
  unsigned spins = 0;
#pragma nounroll
  while (*f < want) { __builtin_amdgcn_s_sleep(2); if (++spins > (SPIN_CAP << 4)) break; }
  CBAR();
}
__device__ __forceinline__ void lds_add(volatile LAS unsigned* f) { __hip_atomic_fetch_add((LAS unsigned*)f, 1u, __ATOMIC_RELAXED, __HIP_MEMORY_SCOPE_WORKGROUP); }

struct Sel { unsigned m0, m1, m2, m3; };
__device__ __forceinline__ Sel mk_sel(int lane) {
  const unsigned h = (lane & 1) ? 0xFFFF0000u : 0x0000FFFFu; const int r = (lane & 7) >> 1;
  return Sel{r == 0 ? h : 0u, r == 1 ? h : 0u, r == 2 ? h : 0u, r == 3 ? h : 0u};
}
__device__ __forceinline__ v4u sel_b(unsigned dup, const Sel& s) { return v4u{dup & s.m0, dup & s.m1, dup & s.m2, dup & s.m3}; }
__device__ __forceinline__ f32x4 strip_fold(f32x4 d) {
  f32x4 y;
#pragma unroll
  for (int i = 0; i < 4; ++i) {
    const float t = dpp<ROR8>(d[i]);
    auto s = __builtin_amdgcn_permlane32_swap(fbits(t), fbits(t), false, false);
    y[i] = d[i] + asf(s[1]);
  }
  return y;
}

struct Ctx {
  LAS unsigned char* L; volatile LAS unsigned* flags; rsrc_t rws;
  int lane, g8, c8, b, xg, idx, kvh, sp;
};
__device__ __forceinline__ void wo_offsets(const Ctx& c, int st, int g8, int c8, unsigned (&off)[2]) {
  const int hrow = (2 * c.kvh + (c.sp >> 3)) * 128 + 32 * ((c.sp >> 1) & 3) + 4 * (c.sp & 1);
#pragma unroll
  for (int t = 0; t < 2; ++t) { const int u = 2 * g8 + t; off[t] = (unsigned)(((hrow + 8 * (u & 3) + (u >> 2)) * D + 64 * st + 8 * c8) * 2); }
}
__device__ __forceinline__ void wd_offsets(const Ctx& c, int st, int g8, int c8, unsigned (&off)[3]) {
  const int odd = c.idx & 1, g0 = 9 * (c.idx >> 1) + 4 * odd;
#pragma unroll
  for (int t = 0; t < 3; ++t) {
    const int u = t < 2 ? 2 * g8 + t : 16 + (g8 & 3), Gi = g0 + (u >> 2);
    const int row = 576 * c.xg + 64 * (Gi >> 4) + 32 * ((Gi >> 3) & 1) + 8 * (u & 3) + (Gi & 7);
    off[t] = (t < 2 || (odd && g8 < 4)) ? (unsigned)((row * D + 64 * st + 8 * c8) * 2) : OOB;
  }
}
__device__ __forceinline__ unsigned strip_off(int lane, int st) {
  unsigned o = (lane < 32 && (lane & 15) < 8) ? (unsigned)((st * 16 + (lane >> 4) * 8 + (lane & 7)) * 16) : 0x80000000u;
  asm volatile("" : "+v"(o));
  return o;
}
__device__ __forceinline__ void publish_at(const Ctx& c, unsigned off, f32x4 acc, const Tag& tag) {
  st_sc1(c.rws, off, pack4(strip_fold(acc), tag));
}
__device__ __forceinline__ void wo_partial(const Ctx& c, unsigned poff, v2u o2, const v4u (&WO)[2], const Sel& sel, const Tag& tag) {
  f32x4 acc = {0.f, 0.f, 0.f, 0.f};
  acc = mfma(WO[0], sel_b(o2.x, sel), acc);
  acc = mfma(WO[1], sel_b(o2.y, sel), acc);
  publish_at(c, poff + WS_SP + c.b * 2304, acc, tag);
  if (c.lane == 0) lds_add(&c.flags[C_SP]);
  PIN();
}
__device__ __forceinline__ void wd_partial(const Ctx& c, unsigned poff, v2u h2, unsigned h3, const v4u (&WD)[3], const Sel& sel, const Tag& tag) {
  f32x4 acc = {0.f, 0.f, 0.f, 0.f};
  acc = mfma(WD[0], sel_b(h2.x, sel), acc);
  acc = mfma(WD[1], sel_b(h2.y, sel), acc);
  acc = mfma(WD[2], sel_b(c.g8 < 4 ? h3 : 0u, sel), acc);
  publish_at(c, poff + WS_FP + c.b * 2304, acc, tag);
  if (c.lane == 0) lds_add(&c.flags[C_FP]);
  PIN();
}

#define STAMP(i) do {} while (0)
#define STAMP_INIT() do {} while (0)
#define STAMP_FLUSH() do {} while (0)

__global__ void __launch_bounds__(NTHR, 1) fused_decode(Params p) {
  extern __shared__ __attribute__((aligned(16))) unsigned char smem[];
  LAS unsigned char* L = (LAS unsigned char*)smem;
  volatile LAS unsigned* flags = (volatile LAS unsigned*)(L + L_FLAG);
  const int lane = threadIdx.x & 63;
  const int wave = UNI(threadIdx.x >> 6);
  const int b = blockIdx.x, xg = b & 7, idx = b >> 3;
  const int kvh = 2 * xg + (idx >> 4), sp = idx & 15;
  const rsrc_t rws = mk_rsrc(p.ws, WS_END);
  const int lg = (idx >> 3) * 8 + xg;
  const int rfin = lg * 8 + (idx & 7);
  const int g8 = lane >> 3, c8 = lane & 7;
  const Ctx c{L, flags, rws, lane, g8, c8, b, xg, idx, kvh, sp};

  unsigned long long code_base;
  asm volatile("s_getpc_b64 %0" : "=s"(code_base));
  code_base &= ~255ull;
  unsigned cpf0 = 0, cpf1 = 0, cpf2 = 0;
  if ((threadIdx.x >> 6) == 10 && (int)(blockIdx.x >> 3) < CODE_PF_WGS) {
    const GAS unsigned* cp = (const GAS unsigned*)code_base + (threadIdx.x & 63) * 32;
    if ((threadIdx.x & 63) * 128 + 16384 < CODE_BYTES) cpf2 = cp[4096];
    if ((threadIdx.x & 63) * 128 + 8192 < CODE_BYTES) cpf1 = cp[2048];
    cpf0 = cp[0];
  }
  PIN();
  STAMP_INIT();
  STAMP(0);
  const unsigned t_start = now32();
  asm volatile("" :: "s"(p.x), "s"(p.kc), "s"(p.vc), "s"(p.wq), "s"(p.wo), "s"(p.wg), "s"(p.wu), "s"(p.wd), "s"(p.out), "s"(p.ws));
  if (wave == 0 && lane < 52) flags[lane] = lane < 16 ? 0u : EMPTY;
  WAIT_LGKM0();
  __builtin_amdgcn_s_barrier();
  PIN();
  STAMP(12);

  unsigned tl0 = 0, tl1 = 0, tl2 = 0, tl3 = 0;
  if (wave == 10) {
    const unsigned E = __hip_atomic_load((const GAS unsigned*)(p.ws + WS_EPOCH), __ATOMIC_RELAXED, __HIP_MEMORY_SCOPE_AGENT);
    const rsrc_t rx = mk_rsrc(p.x, D * 2);
    __builtin_amdgcn_raw_ptr_buffer_load_lds(rx, (LAS void*)(L + L_X), 16, lane * 16, 0, 0, 0);
    __builtin_amdgcn_raw_ptr_buffer_load_lds(rx, (LAS void*)(L + L_X + 1024), 16, 1024 + lane * 16, 0, 0, 0);
    const rsrc_t rq = mk_rsrc(p.wq, D * NQ * 2);
    const unsigned qoff = (unsigned)(((144 * (sp >> 2) + 18 * g8) * NQ + kvh * 256 + 64 * (sp & 3) + 8 * c8) * 2);
#pragma unroll
    for (int t = 0; t < 18; ++t) __builtin_amdgcn_raw_ptr_buffer_load_lds(rq, (LAS void*)(L + L_WQ + 1024 * t), 16, qoff + t * (NQ * 2), 0, 0, AUX_NT);
    PIN();
    {
      const rsrc_t rg = mk_rsrc((idx >> 4) ? p.wu : p.wg, D * F * 2);
      tl0 = __builtin_amdgcn_raw_buffer_load_b32(rg, (TLBP & 1) && lane < 36 ? ((36 * sp + lane) * F + 576 * xg) * 2 : OOB, 0, AUX_NT);
      const int odd = idx & 1, Gi = 9 * (idx >> 1) + 4 * odd + (lane >> 2);
      const int drow = 576 * xg + 64 * (Gi >> 4) + 32 * ((Gi >> 3) & 1) + 8 * (lane & 3) + (Gi & 7);
      tl1 = __builtin_amdgcn_raw_buffer_load_b32(mk_rsrc(p.wd, F * D * 2), (TLBP & 2) && lane < 16 + 4 * odd ? drow * D * 2 : OOB, 0, AUX_NT);
      const int orow = (2 * kvh + (sp >> 3)) * 128 + 32 * ((sp >> 1) & 3) + 4 * (sp & 1) + 8 * (lane & 3) + ((lane >> 2) & 3);
      tl2 = __builtin_amdgcn_raw_buffer_load_b32(mk_rsrc(p.wo, NQ * D * 2), (TLBP & 4) && lane < 16 ? orow * D * 2 : OOB, 0, AUX_NT);
      tl3 = __builtin_amdgcn_raw_buffer_load_b32(mk_rsrc(p.vc + ((size_t)kvh * SK + (SK - WIN)) * DH, WIN * DH * 2), (TLBP & 8) ? (lane * 16 + sp) * 1024 : OOB, 0, AUX_NT);
    }
    PIN();
    STAMP(13);
    __builtin_amdgcn_s_waitcnt(0x4F76);
    const unsigned T = UNI(E) + 1u;
    asm volatile("ds_write_b32 %0, %1 offset:%2\n\ts_waitcnt lgkmcnt(0)\n\tds_write_b32 %0, %3 offset:%4" :: "v"(0u), "v"(T), "i"(L_FLAG + 4 * W_TAG), "v"(1u), "i"(L_FLAG + 4 * F_X) : "memory");
    st_sc1(rws, (idx == 0 && lane == 0) ? (unsigned)(WS_TB + xg * 128) : OOB, pack2x(t_start, 0u, mk_tag(T)));
    __builtin_amdgcn_s_waitcnt(0x0F75);
    asm volatile("ds_write_b32 %0, %1 offset:%2" :: "v"(0u), "v"(1u), "i"(L_FLAG + 4 * F_WQ) : "memory");
    STAMP(14);
  }
  unsigned wave2 = wave;
  asm volatile("" : "+s"(wave2));

  if (wave2 - 1u >= 9u) {
  if (wave2 == 0) {
    const int st = sp & 3, rs = sp >> 2;
    const Sel sel = mk_sel(lane);
    const unsigned qpoff = (lane < 32 && (lane & 15) < 8) ? (unsigned)(WS_QP + ((kvh * 4 + rs) * 4 + st) * 256 + ((lane >> 4) * 8 + c8) * 16) : OOB;
    Tag tag;
    int npass = DRY0 ? 0 : 1; asm volatile("" : "+s"(npass));
#pragma nounroll
    for (int pass = npass; pass < 2; ++pass) {
    if (pass) lds_wait(&flags[F_X], 1u);
    const LAS unsigned* xl = (const LAS unsigned*)(L + L_X);
    const LAS u16* xh = (const LAS u16*)(L + L_X);
    unsigned xw[5];
#pragma unroll
    for (int i = 0; i < 4; ++i) xw[i] = xl[lane + 64 * i];
    xw[4] = lane < 32 ? xl[256 + lane] : 0u;
    const unsigned xq0 = xh[144 * rs + lane], xq1 = xh[144 * rs + 64 + lane], xq2 = xh[144 * rs + 128 + (lane & 15)];
    const unsigned xg0 = xh[36 * sp + (lane < 36 ? lane : 0)];
    const unsigned xr0 = xh[64 * (rfin >> 4) + 32 * ((rfin >> 3) & 1) + 8 * (lane & 3) + (rfin & 7)];
    tag = mk_tag(flags[W_TAG]);
    float xv[10];
#pragma unroll
    for (int i = 0; i < 5; ++i) { xv[2 * i] = asf(xw[i] << 16); xv[2 * i + 1] = asf(xw[i] & 0xffff0000u); }
    const float s = ((xv[0] + xv[1]) + (xv[2] + xv[3])) + ((xv[4] + xv[5]) + (xv[6] + xv[7])) + (xv[8] + xv[9]);
    const float mean = wave_sum_s(s) * (1.f / D);
    float q2 = 0.f, q3 = 0.f;
#pragma unroll
    for (int i = 0; i < 8; i += 2) { const float d0 = xv[i] - mean, d1 = xv[i + 1] - mean; q2 = fmaf(d0, d0, q2); q3 = fmaf(d1, d1, q3); }
    { const float d0 = lane < 32 ? xv[8] - mean : 0.f, d1 = lane < 32 ? xv[9] - mean : 0.f; q2 = fmaf(d0, d0, q2); q3 = fmaf(d1, d1, q3); }
    const float var = wave_sum_s(q2 + q3) * (1.f / D);
    const float mb = rbf(mean);
    const float den = rbf(__builtin_amdgcn_sqrtf(rbf(rbf(var) + 1e-5f)));
    const float rden = __builtin_amdgcn_rcpf(den);
    auto norm = [&](unsigned raw) -> unsigned {
      const float a = rbf(asf(raw << 16) - mb);
      float qv = a * rden; qv = fmaf(fmaf(-qv, den, a), rden, qv);
      return cvtpk(qv, qv);
    };
    LAS unsigned* xq = (LAS unsigned*)(L + L_XQ);
    xq[lane] = norm(xq0); xq[64 + lane] = norm(xq1);
    { const unsigned v2 = norm(xq2); if (lane < 16) xq[128 + lane] = v2; }
    WAIT_LGKM0();
    unsigned xv18[18];
    {
      const LAS unsigned* src = xq + 18 * g8;
#pragma unroll
      for (int i = 0; i < 9; ++i) { const v2u t2 = *(const LAS v2u*)(src + 2 * i); xv18[2 * i] = t2.x; xv18[2 * i + 1] = t2.y; }
    }
    if (pass) STAMP(1);
    f32x4 acc = {0.f, 0.f, 0.f, 0.f};
    if (pass) lds_wait(&flags[F_WQ], 1u);
#pragma unroll
    for (int t = 0; t < 18; ++t) acc = mfma(((const LAS v4u*)(L + L_WQ))[t * 64 + lane], sel_b(xv18[t], sel), acc);
    {
      const f32x4 y = strip_fold(acc);
      st_sc1(rws, pass ? qpoff : OOB, pack4(y, tag));
    }
    if (pass) STAMP(2);
    { const unsigned vg = norm(xg0); if (lane < 40) ((LAS unsigned*)(L + L_XG))[lane] = lane < 36 ? vg : 0u; }
    if (lane < 4) ((LAS float*)(L + L_XR))[lane] = asf(xr0 << 16);
    WAIT_LGKM0();
    if (lane == 0 && pass) flags[F_XN] = 1u;
    }
    {
      unsigned off[4]; bool live[4]; v4u g[4];
#pragma unroll
      for (int r = 0; r < 4; ++r) { off[r] = (unsigned)(WS_QP + (kvh * 4 + r) * 1024 + lane * 16); live[r] = true; }
      __builtin_amdgcn_s_sleep(DLY_Q);
      poll<4>(rws, off, live, tag, g);
      const f32x4 q4 = (asf4(g[0]) + asf4(g[1])) + (asf4(g[2]) + asf4(g[3]));
      LAS u16* qb = (LAS u16*)(L + L_QB) + 64 * (lane >> 4) + 32 * (g8 & 1) + c8;
      const unsigned p01 = cvtpk(q4[0], q4[1]), p23 = cvtpk(q4[2], q4[3]);
      qb[0] = (u16)p01; qb[8] = (u16)(p01 >> 16); qb[16] = (u16)p23; qb[24] = (u16)(p23 >> 16);
      WAIT_LGKM0();
      if (lane == 0) flags[F_Q] = 1u;
    }
    STAMP(3);
    unsigned ooff[2];
    wo_offsets(c, 8, g8, c8, ooff);
    const unsigned poff8 = strip_off(lane, 8);
    const rsrc_t ro = mk_rsrc(p.wo, NQ * D * 2);
    lds_wait_slow(&flags[F_OP], 1u);
    STAMP(4);
    v4u WO[2];
    WO[0] = ld_nt(ro, ooff[0]); WO[1] = ld_nt(ro, ooff[1]);
    PIN();
    {
      const unsigned pb = (unsigned)(WS_OP + (kvh * 16 + (lane & 15)) * 1152);
      unsigned off[2] = {pb + (4 * sp + (lane >> 4)) * 16, pb + (64 + (sp >> 3)) * 16};
      bool live[2] = {true, true}; v4u g[2];
      __builtin_amdgcn_s_sleep(DLY_O);
      poll<2>(rws, off, live, tag, g);
      const float m = asf(g[1].x | (g[1].y >> 10)), l = asf(g[1].z | (g[1].w >> 10));
      const float M = row_max16(m);
      const float wgt = __builtin_amdgcn_exp2f(m - M);
      const float Ls = row_sum16(wgt * l);
      const float o = tr_sum16(wgt * asf4(g[0]), lane) * __builtin_amdgcn_rcpf(Ls);
      if ((lane & 3) == 0) ((LAS unsigned*)(L + L_OS))[lane >> 2] = cvtpk(o, o);
      WAIT_LGKM0();
    }
    STAMP(5);
    wo_partial(c, poff8, *(const LAS v2u*)(L + L_OS + 8 * g8), WO, sel, tag);
    if (lg < 18) {
      lds_wait_slow(&flags[C_SP], 9u);
      __builtin_amdgcn_s_sleep(DLY_S);
      const float sv = final_sum(rws, (unsigned)(WS_SP + rfin * 16), lane, tag);
      if (lane < 16 && (lane & 3) == 0) ((LAS float*)(L + L_FIN))[lane >> 2] = sv;
      WAIT_LGKM0();
      if (lane == 0) flags[F_S] = 1u;
      STAMP(6);
    }
  } else {
    const Tag tag = mk_tag(flags[W_TAG]);
    int late;
    {
      wait_until(t_start + TB_READ);
      const v4u g = untag(ld_sc1(rws, lane < 8 ? (unsigned)(WS_TB + lane * 128) : OOB), tag);
      const bool ok = lane < 8 && (resid(g) & TMASK) == 0u;
      late = ok ? (int)(t_start - (g.x | (g.y >> 10))) : 0;
      late = late > TB_MAX ? 0 : late;
      late = max(late, __builtin_amdgcn_mov_dpp(late, XOR1, 0xf, 0xf, true));
      late = max(late, __builtin_amdgcn_mov_dpp(late, XOR2, 0xf, 0xf, true));
      late = max(late, __builtin_amdgcn_mov_dpp(late, XOR7, 0xf, 0xf, true));
      late = UNI(late);
      if (lane == 0 && late > 0) flags[W_SHIFT] = (unsigned)late;
    }
    asm volatile("" :: "v"(cpf0), "v"(cpf1), "v"(cpf2), "v"(tl0), "v"(tl1), "v"(tl2), "v"(tl3));
    const Sel sel = mk_sel(lane);
    const int g0 = 9 * (idx >> 1) + 4 * (idx & 1), odd = idx & 1;
    unsigned doff[3];
    wd_offsets(c, 8, g8, c8, doff);
    const unsigned poff8 = strip_off(lane, 8);
    const rsrc_t rd = mk_rsrc(p.wd, F * D * 2);
    const bool lc = odd && lane < 32;
    const unsigned pb = (unsigned)(WS_GP + (xg * 32 + (lane & 15)) * 2304);
    unsigned off[3] = {pb + (g0 + (lane >> 4)) * 16, pb + 16 * 2304 + (g0 + (lane >> 4)) * 16, lc ? pb + (lane >> 4) * (16 * 2304) + (g0 + 4) * 16 : OOB};
    bool live[3] = {true, true, lc}; v4u g[3];
    const int vi = (lane >> 2) & 3;
    const int col = 64 * (rfin >> 4) + 32 * ((rfin >> 3) & 1) + 8 * vi + (rfin & 7);
    wait_until(t_start + T_LAST - (unsigned)late);
    v4u WD[3];
#pragma unroll
    for (int t = 0; t < 3; ++t) WD[t] = ld_nt(rd, doff[t]);
    PIN();
    lds_wait(&flags[C_GU], 9u);
    STAMP(1);
    __builtin_amdgcn_s_sleep(DLY_H);
    poll<3>(rws, off, live, tag, g);
    STAMP(2);
    if (!lc) g[2] = v4u{0u, 0u, 0u, 0u};
    {
      const float gs = tr_sum16(asf4(g[0]), lane), us = tr_sum16(asf4(g[1]), lane), cs = tr_sum16(asf4(g[2]), lane);
      auto sw = __builtin_amdgcn_permlane16_swap(fbits(cs), fbits(cs), false, false);
      const bool ex = (lane & 3) == 1;
      const float gin = rbf(ex ? asf(sw[0]) : gs), uin = rbf(ex ? asf(sw[1]) : us);
      const float hv = rbf(silu_f(gin)) * uin;
      LAS unsigned* hs = (LAS unsigned*)(L + L_HS);
      if ((lane & 3) == 0 || (ex && lane < 16)) hs[ex ? 16 + vi : lane >> 2] = cvtpk(hv, hv);
    }
    WAIT_LGKM0();
    STAMP(3);
    wd_partial(c, poff8, *(const LAS v2u*)(L + L_HS + 8 * g8), ((const LAS unsigned*)(L + L_HS))[16 + (g8 & 3)], WD, sel, tag);
    if (lg < 18) {
      const float xr = ((const LAS float*)(L + L_XR))[vi];
      lds_wait(&flags[C_FP], 9u);
      STAMP(4);
      __builtin_amdgcn_s_sleep(DLY_F);
      const float fv = final_sum(rws, (unsigned)(WS_FP + rfin * 16), lane, tag);
      STAMP(5);
      const float gate = rbf(silu_f(rbf(fv)));
      lds_wait(&flags[F_S], 1u);
      const float sv = ((const LAS float*)(L + L_FIN))[vi];
      const float res = xr + rbf(rbf(sv) * gate);
      if (lane < 16 && (lane & 3) == 0) p.out[col] = (u16)cvtpk(res, res);
      if (b == 0 && lane == 0) __hip_atomic_store((GAS unsigned*)(p.ws + WS_EPOCH), flags[W_TAG], __ATOMIC_RELAXED, __HIP_MEMORY_SCOPE_AGENT);
      STAMP(6);
    }
  }
  } else {
  const int st = wave - 1;
  const Sel sel = mk_sel(lane);
  const rsrc_t rg = mk_rsrc((idx >> 4) ? p.wu : p.wg, D * F * 2);
  unsigned goff, goff4, ooff[2], doff[3];
  auto late_addr = [&]() {
    PIN();
    int ln = lane; asm volatile("" : "+v"(ln));
    const int g8_ = ln >> 3, c8_ = ln & 7;
    const unsigned gcol = (unsigned)(576 * xg + 64 * st + 8 * c8_);
    goff = (unsigned)(((36 * sp + 4 * g8_) * F + gcol) * 2);
    goff4 = g8_ < 4 ? (unsigned)(((36 * sp + 32 + g8_) * F + gcol) * 2) : OOB;
    wo_offsets(c, st, g8_, c8_, ooff);
    wd_offsets(c, st, g8_, c8_, doff);
    PIN();
  };
  const unsigned poff = strip_off(lane, st);
  v4u G[5];
  unsigned tn;
#define SLOT(dt) do { wait_until(tn); tn += (dt); } while (0)
  auto issue_g = [&](int t) { SLOT(DT2); G[t] = ld_nt(rg, t < 4 ? goff + t * (F * 2) : goff4); PIN(); };
  v4u gb[5];
  auto gu_frags = [&]() {
    const v4u x4 = *(const LAS v4u*)(L + L_XG + 16 * g8);
    const unsigned x5 = ((const LAS unsigned*)(L + L_XG))[32 + g8];
#pragma unroll
    for (int t = 0; t < 4; ++t) gb[t] = sel_b(x4[t], sel);
    gb[4] = sel_b(x5, sel);
    PIN();
  };
  auto do_gu = [&](const Tag& tag) {
    f32x4 acc = {0.f, 0.f, 0.f, 0.f};
#pragma unroll
    for (int t = 0; t < 5; ++t) acc = mfma(G[t], gb[t], acc);
    publish_at(c, poff + WS_GP + (xg * 32 + idx) * 2304, acc, tag);
    if (lane == 0) lds_add(&flags[C_GU]);
    PIN();
  };

  if (wave > 8) {
    tn = now32() + TSTART + 2 * DT1;
    late_addr();
#pragma unroll
    for (int t = 0; t < 5; ++t) { issue_g(t); tn += W9GAP - DT2; }
    lds_wait_slow(&flags[F_XN], 1u);
    const Tag tag = mk_tag(flags[W_TAG]);
    gu_frags();
    do_gu(tag);
    STAMP(2);
    lds_wait(&flags[C_ATT], 8u);
    STAMP(3);
    const int hh = g8 & 1;
    f32x2 ml[8]; f32x4 po[8];
#pragma unroll
    for (int w = 0; w < 8; ++w) { ml[w] = ((const LAS f32x2*)(L + L_ML))[w * 2 + hh]; po[w] = ((const LAS f32x4*)(L + L_PO))[w * 64 + lane]; }
    const float M = fmaxf(fmaxf(fmaxf(ml[0].x, ml[1].x), fmaxf(ml[2].x, ml[3].x)), fmaxf(fmaxf(ml[4].x, ml[5].x), fmaxf(ml[6].x, ml[7].x)));
    f32x4 o = {0.f, 0.f, 0.f, 0.f}; float Ls = 0.f;
#pragma unroll
    for (int w = 0; w < 8; ++w) {
      const float sc = __builtin_amdgcn_exp2f(ml[w].x - M);
      Ls = fmaf(sc, ml[w].y, Ls);
      o += sc * po[w];
    }
    const unsigned pbase = (unsigned)(WS_OP + (kvh * 16 + sp) * 1152);
    st_sc1(rws, pbase + (hh * 32 + (lane >> 4) * 8 + c8) * 16, pack4(o, tag));
    if ((lane & 0x37) == 0) st_sc1(rws, pbase + (64 + hh) * 16, pack2x(fbits(M), fbits(Ls), tag));
    if (lane == 0) flags[F_OP] = 1u;
    PIN();
    STAMP(4);
  } else {
    const size_t tile = ((size_t)kvh * SK + (SK - WIN)) * DH;
    const rsrc_t rk = mk_rsrc(p.kc + tile, WIN * DH * 2);
    const rsrc_t rv = mk_rsrc(p.vc + tile, WIN * DH * 2);
    const int cbase = (st * 8 * 16 + sp) * 1024;
    const unsigned koff = (unsigned)(((lane >> 2) & 3) * 256 + ((lane & 3) + 4 * (lane >> 4)) * 16);
    STAMP(13);
    tn = now32() + TSTART + (st * DT1) / 8;
    v4u K[8], V[8];
#define LDK(t) do { SLOT(DT1); if (t >= KWIN) WAIT_VM(KWIN - 1); K[t] = __builtin_amdgcn_raw_buffer_load_b128(rk, (int)koff, cbase + t * 16384, AUX_NT); PIN(); } while (0)
#define LDV(t) do { SLOT(DT1); WAIT_VM(KWIN - 1); V[t] = __builtin_amdgcn_raw_buffer_load_b128(rv, lane * 16, cbase + t * 16384, AUX_NT); PIN(); } while (0)
    unsigned shifted = 0;
#define CATCH_UP() do { const unsigned sh_ = UNI(flags[W_SHIFT]); tn -= sh_ - shifted; shifted = sh_; } while (0)
#pragma unroll
    for (int t = 0; t < 8; ++t) { LDK(t); if (t == 1) late_addr(); if (t == 5) CATCH_UP(); }
    constexpr float C2 = 0.08838834764831845f * 1.4426950408889634f;
    unsigned pk[8];
    auto qk_softmax = [&]() {
      const v4u qf = *(const LAS v4u*)(L + L_QB + ((g8 & 1) * 128 + ((lane & 3) + 4 * (lane >> 4)) * 8) * 2);
      float s[8];
#pragma unroll
      for (int t = 0; t < 8; ++t) {
        const f32x4 d = mfma(K[t], qf, f32x4{0.f, 0.f, 0.f, 0.f});
        s[t] = (dpp<0x00>(d[0]) + dpp<0x55>(d[1])) + (dpp<0xAA>(d[2]) + dpp<0xFF>(d[3]));
      }
      float m = fmaxf(fmaxf(fmaxf(s[0], s[1]), fmaxf(s[2], s[3])), fmaxf(fmaxf(s[4], s[5]), fmaxf(s[6], s[7])));
      m = x16_max(m) * C2;
      float l = 0.f;
#pragma unroll
      for (int t = 0; t < 8; ++t) { const float pr = __builtin_amdgcn_exp2f(fmaf(s[t], C2, -m)); l += pr; pk[t] = cvtpk(pr, pr); }
      l = x16_sum(l);
      if ((lane & 0x37) == 0) ((LAS f32x2*)(L + L_ML))[st * 2 + g8] = f32x2{m, l};
      PIN();
    };
#pragma unroll
    for (int t = 0; t < QKPOS; ++t) { LDV(t); if (t == 0 || t == 3) CATCH_UP(); }
    const bool early = UNI(flags[F_Q]) != 0u;
    if (early) qk_softmax();
    STAMP(3);
#pragma unroll
    for (int t = QKPOS; t < 8; ++t) LDV(t);
    STAMP(11);
    tn -= (st * DT1) / 8;
#pragma unroll
    for (int t = 0; t < QKG; ++t) issue_g(t);
    STAMP(1);
    if (!early) { lds_wait(&flags[F_Q], 1u); qk_softmax(); }
    STAMP(2);
    f32x4 oacc = {0.f, 0.f, 0.f, 0.f};
#pragma unroll
    for (int t = 0; t < 8; ++t) oacc = mfma(V[t], sel_b(pk[t], sel), oacc);
    ((LAS f32x4*)(L + L_PO))[st * 64 + lane] = oacc;
    WAIT_LGKM0();
    if (lane == 0) lds_add(&flags[C_ATT]);
    PIN();
    STAMP(4);
#pragma unroll
    for (int t = QKG; t < 5; ++t) issue_g(t);
    const Tag tag = mk_tag(flags[W_TAG]);
    gu_frags();
    const rsrc_t ro = mk_rsrc(p.wo, NQ * D * 2);
    const rsrc_t rd = mk_rsrc(p.wd, F * D * 2);
    v4u WO[2], WD[3];
    f32x4 gacc = {0.f, 0.f, 0.f, 0.f};
#pragma unroll
    for (int t = 0; t < 3; ++t) gacc = mfma(G[t], gb[t], gacc);
    PIN();
    tn += DTD - DT2;
    SLOT(DTD); WD[0] = ld_nt(rd, doff[0]); PIN();
    STAMP(5);
    gacc = mfma(G[3], gb[3], gacc);
    gacc = mfma(G[4], gb[4], gacc);
    publish_at(c, poff + WS_GP + (xg * 32 + idx) * 2304, gacc, tag);
    if (lane == 0) lds_add(&flags[C_GU]);
    PIN();
    STAMP(6);
    SLOT(DTD); WD[1] = ld_nt(rd, doff[1]); PIN();
    if (idx & 1) SLOT(DTD);
    WD[2] = ld_nt(rd, doff[2]); PIN();
    tn += DTO - DTD;
    SLOT(DT2); WO[0] = ld_nt(ro, ooff[0]); PIN();
    SLOT(DT2); WO[1] = ld_nt(ro, ooff[1]); PIN();
    v2u h2, o2; unsigned h3, spins = 0; bool hh;
#pragma nounroll
    for (;;) {
      h2 = *(const volatile LAS v2u*)(L + L_HS + 8 * g8); h3 = ((const volatile LAS unsigned*)(L + L_HS))[16 + (g8 & 3)];
      o2 = *(const volatile LAS v2u*)(L + L_OS + 8 * g8);
      hh = __all(h2.x != EMPTY && h2.y != EMPTY && h3 != EMPTY);
      if (hh || __all(o2.x != EMPTY && o2.y != EMPTY) || ++spins > (SPIN_CAP << 6)) break;
    }
    if (hh) {
      wd_partial(c, poff, h2, h3, WD, sel, tag);
      STAMP(8);
#pragma nounroll
      while (!__all(o2.x != EMPTY && o2.y != EMPTY) && ++spins <= (SPIN_CAP << 6)) o2 = *(const volatile LAS v2u*)(L + L_OS + 8 * g8);
      wo_partial(c, poff, o2, WO, sel, tag);
      STAMP(7);
    } else {
      wo_partial(c, poff, o2, WO, sel, tag);
      STAMP(7);
#pragma nounroll
      while (!__all(h2.x != EMPTY && h2.y != EMPTY && h3 != EMPTY) && ++spins <= (SPIN_CAP << 6)) {
        h2 = *(const volatile LAS v2u*)(L + L_HS + 8 * g8); h3 = ((const volatile LAS unsigned*)(L + L_HS))[16 + (g8 & 3)];
      }
      wd_partial(c, poff, h2, h3, WD, sel, tag);
      STAMP(8);
    }
  }
  }
  STAMP_FLUSH();
}

}

extern "C" void kernel_launch(void* const* d_in, const int* in_sizes, int n_in, void* d_out, int out_size, void* d_ws, size_t ws_size, hipStream_t stream) {
  Params p;
  p.x = (const u16*)d_in[0]; p.kc = (const u16*)d_in[1]; p.vc = (const u16*)d_in[2];
  p.wq = (const u16*)d_in[3]; p.wo = (const u16*)d_in[4];
  p.wg = (const u16*)d_in[6]; p.wu = (const u16*)d_in[7]; p.wd = (const u16*)d_in[8];
  p.out = (u16*)d_out; p.ws = (unsigned char*)d_ws;
  (void)hipFuncSetAttribute((const void*)fused_decode, hipFuncAttributeMaxDynamicSharedMemorySize, LDS_BYTES);
  hipLaunchKernelGGL(fused_decode, dim3(NWG), dim3(NTHR), LDS_BYTES, stream, p);
}
```

```cpp
#include <hip/hip_runtime.h>
#include <hip/hip_bf16.h>
#include <stdint.h>

#define GAS __attribute__((address_space(1)))
#define LAS __attribute__((address_space(3)))
typedef unsigned v4u __attribute__((ext_vector_type(4)));
typedef unsigned v2u __attribute__((ext_vector_type(2)));
typedef float f32x4 __attribute__((ext_vector_type(4)));
typedef float f32x2 __attribute__((ext_vector_type(2)));
typedef short bf16x8 __attribute__((ext_vector_type(8)));
typedef __bf16 bf16x2 __attribute__((ext_vector_type(2)));
typedef unsigned short u16;
typedef __amdgpu_buffer_rsrc_t rsrc_t;

#define KWIN 4
#define DT1 34
#define DT2 36
#define DTD 36
#define DTO 36
#define DRY0 0
#define OUTP 1
#define TLBP 0
#define TSTART 10
#define QKPOS 6
#define QKG 3
#define W9GAP 70
#define T_LAST 790
#define CODE_BYTES 17920
#define CODE_PF_WGS 2
#define TB_READ 170
#define TB_MAX 300
#define DLY_Q 36
#define DLY_O 30
#define DLY_H 20
#define DLY_F 20
#define DLY_S 28
#define RETRY 8

namespace {

constexpr int D = 576, SK = 65536, DH = 128, WIN = 4096, F = 4608, NQ = 4096;
constexpr int NWG = 256, NWAVE = 11, NTHR = NWAVE * 64;
constexpr int LDS_BYTES = 84 * 1024;

constexpr int WS_EPOCH = 0;
constexpr int WS_TB = 256;
constexpr int WS_QP = WS_TB + 1024;
constexpr int WS_OP = WS_QP + 16 * 4096;
constexpr int WS_GP = WS_OP + 16 * 16 * 1152;
constexpr int WS_SP = WS_GP + 8 * 32 * 2304;
constexpr int WS_FP = WS_SP + 256 * 2304;
constexpr int WS_END = WS_FP + 256 * 2304;

constexpr int L_FLAG = 0;
constexpr int L_OS = 64;
constexpr int L_HS = L_OS + 64;
constexpr int L_XQ = L_HS + 128;
constexpr int L_XG = L_XQ + 576;
constexpr int L_XR = L_XG + 160;
constexpr int L_QB = L_XR + 16;
constexpr int L_ML = L_QB + 512;
constexpr int L_FIN = L_ML + 128;
constexpr int L_PO = L_FIN + 16;
constexpr int L_X = L_PO + 8192;
constexpr int L_WQ = L_X + 2048;
constexpr int L_TS = L_WQ + 18 * 1024;
static_assert(L_PO % 16 == 0 && L_XQ % 16 == 0 && L_XG % 16 == 0 && L_QB % 16 == 0 && L_OS % 16 == 0 && L_HS % 16 == 0 && L_XR % 16 == 0 && L_X % 16 == 0, "align");
enum { F_XN = 0, F_Q = 1, C_ATT = 2, C_GU = 3, F_OP = 4, C_SP = 5, C_FP = 6, F_S = 7, W_TAG = 8, W_SHIFT = 9, F_X = 10, F_WQ = 11 };

constexpr int AUX_NT = 2, AUX_SC1 = 16;
constexpr unsigned VMASK = 0xFFFFFC00u, TMASK = 0x3FFu, OOB = 0xFFFFFF00u;
constexpr unsigned EMPTY = 0xFFFF0000u;
constexpr unsigned SPIN_CAP = 1u << 17;

struct Params {
  const u16* x; const u16* kc; const u16* vc; const u16* wq; const u16* wo;
  const u16* wg; const u16* wu; const u16* wd; u16* out; unsigned char* ws;
};

__device__ __forceinline__ unsigned fbits(float f) { return __builtin_bit_cast(unsigned, f); }
__device__ __forceinline__ float asf(unsigned u) { return __builtin_bit_cast(float, u); }
__device__ __forceinline__ unsigned cvtpk(float a, float b) { bf16x2 r = {(__bf16)a, (__bf16)b}; return __builtin_bit_cast(unsigned, r); }
__device__ __forceinline__ float rbf(float f) { return asf(cvtpk(0.f, f) & 0xffff0000u); }
__device__ __forceinline__ float silu_f(float x) { return x * __builtin_amdgcn_rcpf(1.f + __builtin_amdgcn_exp2f(x * -1.4426950408889634f)); }
__device__ __forceinline__ f32x4 mfma(v4u a, v4u b, f32x4 c) {
  return __builtin_amdgcn_mfma_f32_16x16x32_bf16(__builtin_bit_cast(bf16x8, a), __builtin_bit_cast(bf16x8, b), c, 0, 0, 0);
}
template <int CTRL> __device__ __forceinline__ float dpp(float x) {
  return asf(__builtin_amdgcn_mov_dpp(__builtin_bit_cast(int, x), CTRL, 0xf, 0xf, true));
}
template <int CTRL, int RM> __device__ __forceinline__ float dppm(float x) {
  return asf(__builtin_amdgcn_update_dpp(0, __builtin_bit_cast(int, x), CTRL, RM, 0xf, true));
}
constexpr int XOR1 = 0xB1, XOR2 = 0x4E, XOR7 = 0x141, ROR8 = 0x128;
__device__ __forceinline__ float x16_max(float x) {
  auto s = __builtin_amdgcn_permlane16_swap(fbits(x), fbits(x), false, false);
  x = fmaxf(asf(s[0]), asf(s[1]));
  auto t = __builtin_amdgcn_permlane32_swap(fbits(x), fbits(x), false, false);
  return fmaxf(asf(t[0]), asf(t[1]));
}
__device__ __forceinline__ float x16_sum(float x) {
  auto s = __builtin_amdgcn_permlane16_swap(fbits(x), fbits(x), false, false);
  x = asf(s[0]) + asf(s[1]);
  auto t = __builtin_amdgcn_permlane32_swap(fbits(x), fbits(x), false, false);
  return asf(t[0]) + asf(t[1]);
}
__device__ __forceinline__ float row_sum16(float x) { x += dpp<XOR1>(x); x += dpp<XOR2>(x); x += dpp<XOR7>(x); x += dpp<ROR8>(x); return x; }
__device__ __forceinline__ float row_max16(float x) { x = fmaxf(x, dpp<XOR1>(x)); x = fmaxf(x, dpp<XOR2>(x)); x = fmaxf(x, dpp<XOR7>(x)); x = fmaxf(x, dpp<ROR8>(x)); return x; }
__device__ __forceinline__ float wave_sum_s(float s) {
  s += dpp<0x111>(s); s += dpp<0x112>(s); s += dpp<0x114>(s); s += dpp<0x118>(s);
  s += dppm<0x142, 0xa>(s); s += dppm<0x143, 0xc>(s);
  return asf(__builtin_amdgcn_readlane(fbits(s), 63));
}
__device__ __forceinline__ float tr_sum16(f32x4 v, int lane) {
  const bool b3 = lane & 8, b2 = lane & 4;
  const float a = (b3 ? v[2] : v[0]) + dpp<ROR8>(b3 ? v[0] : v[2]);
  const float b = (b3 ? v[3] : v[1]) + dpp<ROR8>(b3 ? v[1] : v[3]);
  float r = (b2 ? b : a) + dpp<XOR7>(b2 ? a : b);
  r += dpp<XOR2>(r); r += dpp<XOR1>(r);
  return r;
}

__device__ __forceinline__ rsrc_t mk_rsrc(const void* p, unsigned bytes) {
  return __builtin_amdgcn_make_buffer_rsrc((void*)p, 0, (int)bytes, 0x00020000);
}
__device__ __forceinline__ v4u ld_nt(rsrc_t r, unsigned off) { return __builtin_amdgcn_raw_buffer_load_b128(r, (int)off, 0, AUX_NT); }
__device__ __forceinline__ v4u ld_sc1(rsrc_t r, unsigned off) { return __builtin_amdgcn_raw_buffer_load_b128(r, (int)off, 0, AUX_SC1); }
__device__ __forceinline__ void st_sc1(rsrc_t r, unsigned off, v4u g) { __builtin_amdgcn_raw_buffer_store_b128(g, r, (int)off, 0, AUX_SC1); }
__device__ __forceinline__ unsigned now32() { unsigned long long t; asm volatile("s_memrealtime %0\n\ts_waitcnt lgkmcnt(0)" : "=s"(t) :: "memory"); return (unsigned)t; }
__device__ __forceinline__ void wait_until(unsigned t) {
#pragma nounroll
  while ((int)(now32() - t) < 0) __builtin_amdgcn_s_sleep(1);
}
#define PIN() __builtin_amdgcn_sched_barrier(0)
#define CBAR() asm volatile("" ::: "memory")
#define WAIT_VM(n) asm volatile("s_waitcnt vmcnt(%0)" ::"i"(n) : "memory")
#define WAIT_LGKM0() asm volatile("s_waitcnt lgkmcnt(0)" ::: "memory")
#define UNI(x) __builtin_amdgcn_readfirstlane(x)

struct Tag { unsigned t0, t1, t2, t3; };
__device__ __forceinline__ Tag mk_tag(unsigned T) { return Tag{T & TMASK, (T + 0x155u) & TMASK, (T + 0x2AAu) & TMASK, (T + 0x0FFu) & TMASK}; }
__device__ __forceinline__ v4u pack4(f32x4 v, const Tag& t) {
  return v4u{(fbits(v.x) & VMASK) | t.t0, (fbits(v.y) & VMASK) | t.t1, (fbits(v.z) & VMASK) | t.t2, (fbits(v.w) & VMASK) | t.t3};
}
__device__ __forceinline__ v4u pack2x(unsigned a, unsigned b, const Tag& t) {
  return v4u{(a & VMASK) | t.t0, ((a & TMASK) << 10) | t.t1, (b & VMASK) | t.t2, ((b & TMASK) << 10) | t.t3};
}
__device__ __forceinline__ v4u untag(v4u g, const Tag& t) { return v4u{g.x ^ t.t0, g.y ^ t.t1, g.z ^ t.t2, g.w ^ t.t3}; }
__device__ __forceinline__ unsigned resid(v4u u) { return (u.x | u.y | u.z) | u.w; }
__device__ __forceinline__ f32x4 asf4(v4u u) { return __builtin_bit_cast(f32x4, u); }

template <int N>
__device__ __forceinline__ void poll(rsrc_t r, const unsigned (&off)[N], const bool (&live)[N], const Tag& tag, v4u (&res)[N]) {
  unsigned spins = 0;
#pragma nounroll
  for (;;) {
    v4u a[N];
#pragma unroll
    for (int i = 0; i < N; ++i) a[i] = ld_sc1(r, off[i]);
    CBAR();
    unsigned bad = 0;
#pragma unroll
    for (int i = 0; i < N; ++i) { res[i] = untag(a[i], tag); bad |= live[i] ? resid(res[i]) : 0u; }
    if (__all((bad & TMASK) == 0u) || ++spins > SPIN_CAP) break;
    __builtin_amdgcn_s_sleep(RETRY);
  }
}
__device__ __forceinline__ float final_sum(rsrc_t rws, unsigned base, int lane, const Tag& tag) {
  unsigned off[4]; bool live[4]; v4u g[4];
#pragma unroll
  for (int i = 0; i < 4; ++i) { off[i] = base + (unsigned)((i * 64 + lane) * 2304); live[i] = true; }
  poll<4>(rws, off, live, tag, g);
  const f32x4 v = (asf4(g[0]) + asf4(g[1])) + (asf4(g[2]) + asf4(g[3]));
  return x16_sum(tr_sum16(v, lane));
}

__device__ __forceinline__ void lds_wait(volatile LAS unsigned* f, unsigned want) {
  unsigned spins = 0;
#pragma nounroll
  while (*f < want) { if (++spins > (SPIN_CAP << 6)) break; }
  CBAR();
}
__device__ __forceinline__ void lds_wait_slow(volatile LAS unsigned* f, unsigned want) {
  unsigned spins = 0;
#pragma nounroll
  while (*f < want) { __builtin_amdgcn_s_sleep(2); if (++spins > (SPIN_CAP << 4)) break; }
  CBAR();
}
__device__ __forceinline__ void lds_add(volatile LAS unsigned* f) { __hip_atomic_fetch_add((LAS unsigned*)f, 1u, __ATOMIC_RELAXED, __HIP_MEMORY_SCOPE_WORKGROUP); }

struct Sel { unsigned m0, m1, m2, m3; };
__device__ __forceinline__ Sel mk_sel(int lane) {
  const unsigned h = (lane & 1) ? 0xFFFF0000u : 0x0000FFFFu; const int r = (lane & 7) >> 1;
  return Sel{r == 0 ? h : 0u, r == 1 ? h : 0u, r == 2 ? h : 0u, r == 3 ? h : 0u};
}
__device__ __forceinline__ v4u sel_b(unsigned dup, const Sel& s) { return v4u{dup & s.m0, dup & s.m1, dup & s.m2, dup & s.m3}; }
__device__ __forceinline__ f32x4 strip_fold(f32x4 d) {
  f32x4 y;
#pragma unroll
  for (int i = 0; i < 4; ++i) {
    const float t = dpp<ROR8>(d[i]);
    auto s = __builtin_amdgcn_permlane32_swap(fbits(t), fbits(t), false, false);
    y[i] = d[i] + asf(s[1]);
  }
  return y;
}

struct Ctx {
  LAS unsigned char* L; volatile LAS unsigned* flags; rsrc_t rws;
  int lane, g8, c8, b, xg, idx, kvh, sp;
};
__device__ __forceinline__ void wo_offsets(const Ctx& c, int st, int g8, int c8, unsigned (&off)[2]) {
  const int hrow = (2 * c.kvh + (c.sp >> 3)) * 128 + 32 * ((c.sp >> 1) & 3) + 4 * (c.sp & 1);
#pragma unroll
  for (int t = 0; t < 2; ++t) { const int u = 2 * g8 + t; off[t] = (unsigned)(((hrow + 8 * (u & 3) + (u >> 2)) * D + 64 * st + 8 * c8) * 2); }
}
__device__ __forceinline__ void wd_offsets(const Ctx& c, int st, int g8, int c8, unsigned (&off)[3]) {
  const int odd = c.idx & 1, g0 = 9 * (c.idx >> 1) + 4 * odd;
#pragma unroll
  for (int t = 0; t < 3; ++t) {
    const int u = t < 2 ? 2 * g8 + t : 16 + (g8 & 3), Gi = g0 + (u >> 2);
    const int row = 576 * c.xg + 64 * (Gi >> 4) + 32 * ((Gi >> 3) & 1) + 8 * (u & 3) + (Gi & 7);
    off[t] = (t < 2 || (odd && g8 < 4)) ? (unsigned)((row * D + 64 * st + 8 * c8) * 2) : OOB;
  }
}
__device__ __forceinline__ unsigned strip_off(int lane, int st) {
  unsigned o = (lane < 32 && (lane & 15) < 8) ? (unsigned)((st * 16 + (lane >> 4) * 8 + (lane & 7)) * 16) : 0x80000000u;
  asm volatile("" : "+v"(o));
  return o;
}
__device__ __forceinline__ void publish_at(const Ctx& c, unsigned off, f32x4 acc, const Tag& tag) {
  st_sc1(c.rws, off, pack4(strip_fold(acc), tag));
}
__device__ __forceinline__ void wo_partial(const Ctx& c, unsigned poff, v2u o2, const v4u (&WO)[2], const Sel& sel, const Tag& tag) {
  f32x4 acc = {0.f, 0.f, 0.f, 0.f};
  acc = mfma(WO[0], sel_b(o2.x, sel), acc);
  acc = mfma(WO[1], sel_b(o2.y, sel), acc);
  publish_at(c, poff + WS_SP + c.b * 2304, acc, tag);
  if (c.lane == 0) lds_add(&c.flags[C_SP]);
  PIN();
}
__device__ __forceinline__ void wd_partial(const Ctx& c, unsigned poff, v2u h2, unsigned h3, const v4u (&WD)[3], const Sel& sel, const Tag& tag) {
  f32x4 acc = {0.f, 0.f, 0.f, 0.f};
  acc = mfma(WD[0], sel_b(h2.x, sel), acc);
  acc = mfma(WD[1], sel_b(h2.y, sel), acc);
  acc = mfma(WD[2], sel_b(c.g8 < 4 ? h3 : 0u, sel), acc);
  publish_at(c, poff + WS_FP + c.b * 2304, acc, tag);
  if (c.lane == 0) lds_add(&c.flags[C_FP]);
  PIN();
}

#define STAMP(i) do {} while (0)
#define STAMP_INIT() do {} while (0)
#define STAMP_FLUSH() do {} while (0)

__global__ void __launch_bounds__(NTHR, 1) fused_decode(Params p) {
  extern __shared__ __attribute__((aligned(16))) unsigned char smem[];
  LAS unsigned char* L = (LAS unsigned char*)smem;
  volatile LAS unsigned* flags = (volatile LAS unsigned*)(L + L_FLAG);
  const int lane = threadIdx.x & 63;
  const int wave = UNI(threadIdx.x >> 6);
  const int b = blockIdx.x, xg = b & 7, idx = b >> 3;
  const int kvh = 2 * xg + (idx >> 4), sp = idx & 15;
  const rsrc_t rws = mk_rsrc(p.ws, WS_END);
  const int lg = (idx >> 3) * 8 + xg;
  const int rfin = lg * 8 + (idx & 7);
  const int g8 = lane >> 3, c8 = lane & 7;
  const Ctx c{L, flags, rws, lane, g8, c8, b, xg, idx, kvh, sp};

  unsigned long long code_base;
  asm volatile("s_getpc_b64 %0" : "=s"(code_base));
  code_base &= ~255ull;
  unsigned cpf0 = 0, cpf1 = 0, cpf2 = 0;
  if ((threadIdx.x >> 6) == 10 && (int)(blockIdx.x >> 3) < CODE_PF_WGS) {
    const GAS unsigned* cp = (const GAS unsigned*)code_base + (threadIdx.x & 63) * 32;
    if ((threadIdx.x & 63) * 128 + 16384 < CODE_BYTES) cpf2 = cp[4096];
    if ((threadIdx.x & 63) * 128 + 8192 < CODE_BYTES) cpf1 = cp[2048];
    cpf0 = cp[0];
  }
  PIN();
  STAMP_INIT();
  STAMP(0);
  const unsigned t_start = now32();
  asm volatile("" :: "s"(p.x), "s"(p.kc), "s"(p.vc), "s"(p.wq), "s"(p.wo), "s"(p.wg), "s"(p.wu), "s"(p.wd), "s"(p.out), "s"(p.ws));
  if (wave == 0 && lane < 52) flags[lane] = lane < 16 ? 0u : EMPTY;
  WAIT_LGKM0();
  __builtin_amdgcn_s_barrier();
  PIN();
  STAMP(12);

  unsigned tl0 = 0, tl1 = 0, tl2 = 0, tl3 = 0;
  if (wave == 10) {
    const unsigned E = __hip_atomic_load((const GAS unsigned*)(p.ws + WS_EPOCH), __ATOMIC_RELAXED, __HIP_MEMORY_SCOPE_AGENT);
    const rsrc_t rx = mk_rsrc(p.x, D * 2);
    __builtin_amdgcn_raw_ptr_buffer_load_lds(rx, (LAS void*)(L + L_X), 16, lane * 16, 0, 0, 0);
    __builtin_amdgcn_raw_ptr_buffer_load_lds(rx, (LAS void*)(L + L_X + 1024), 16, 1024 + lane * 16, 0, 0, 0);
    const rsrc_t rq = mk_rsrc(p.wq, D * NQ * 2);
    const unsigned qoff = (unsigned)(((144 * (sp >> 2) + 18 * g8) * NQ + kvh * 256 + 64 * (sp & 3) + 8 * c8) * 2);
#pragma unroll
    for (int t = 0; t < 18; ++t) __builtin_amdgcn_raw_ptr_buffer_load_lds(rq, (LAS void*)(L + L_WQ + 1024 * t), 16, qoff + t * (NQ * 2), 0, 0, AUX_NT);
    PIN();
    {
      const rsrc_t rg = mk_rsrc((idx >> 4) ? p.wu : p.wg, D * F * 2);
      tl0 = __builtin_amdgcn_raw_buffer_load_b32(rg, (TLBP & 1) && lane < 36 ? ((36 * sp + lane) * F + 576 * xg) * 2 : OOB, 0, AUX_NT);
      const int odd = idx & 1, Gi = 9 * (idx >> 1) + 4 * odd + (lane >> 2);
      const int drow = 576 * xg + 64 * (Gi >> 4) + 32 * ((Gi >> 3) & 1) + 8 * (lane & 3) + (Gi & 7);
      tl1 = __builtin_amdgcn_raw_buffer_load_b32(mk_rsrc(p.wd, F * D * 2), (TLBP & 2) && lane < 16 + 4 * odd ? drow * D * 2 : OOB, 0, AUX_NT);
      const int orow = (2 * kvh + (sp >> 3)) * 128 + 32 * ((sp >> 1) & 3) + 4 * (sp & 1) + 8 * (lane & 3) + ((lane >> 2) & 3);
      tl2 = __builtin_amdgcn_raw_buffer_load_b32(mk_rsrc(p.wo, NQ * D * 2), (TLBP & 4) && lane < 16 ? orow * D * 2 : OOB, 0, AUX_NT);
      tl3 = __builtin_amdgcn_raw_buffer_load_b32(mk_rsrc(p.vc + ((size_t)kvh * SK + (SK - WIN)) * DH, WIN * DH * 2), (TLBP & 8) ? (lane * 16 + sp) * 1024 : OOB, 0, AUX_NT);
    }
    __builtin_amdgcn_raw_buffer_store_b16((unsigned short)0, mk_rsrc(p.out, D * 2),
                                          (OUTP && lg < 18 && lane < 4) ? (64 * (rfin >> 4) + 32 * ((rfin >> 3) & 1) + 8 * lane + (rfin & 7)) * 2 : (int)OOB, 0, 0);
    PIN();
    STAMP(13);
    __builtin_amdgcn_s_waitcnt(0x4F77);
    const unsigned T = UNI(E) + 1u;
    asm volatile("ds_write_b32 %0, %1 offset:%2\n\ts_waitcnt lgkmcnt(0)\n\tds_write_b32 %0, %3 offset:%4" :: "v"(0u), "v"(T), "i"(L_FLAG + 4 * W_TAG), "v"(1u), "i"(L_FLAG + 4 * F_X) : "memory");
    st_sc1(rws, (idx == 0 && lane == 0) ? (unsigned)(WS_TB + xg * 128) : OOB, pack2x(t_start, 0u, mk_tag(T)));
    __builtin_amdgcn_s_waitcnt(0x0F76);
    asm volatile("ds_write_b32 %0, %1 offset:%2" :: "v"(0u), "v"(1u), "i"(L_FLAG + 4 * F_WQ) : "memory");
    STAMP(14);
  }
  unsigned wave2 = wave;
  asm volatile("" : "+s"(wave2));

  if (wave2 - 1u >= 9u) {
  if (wave2 == 0) {
    const int st = sp & 3, rs = sp >> 2;
    const Sel sel = mk_sel(lane);
    const unsigned qpoff = (lane < 32 && (lane & 15) < 8) ? (unsigned)(WS_QP + ((kvh * 4 + rs) * 4 + st) * 256 + ((lane >> 4) * 8 + c8) * 16) : OOB;
    Tag tag;
    int npass = DRY0 ? 0 : 1; asm volatile("" : "+s"(npass));
#pragma nounroll
    for (int pass = npass; pass < 2; ++pass) {
    if (pass) lds_wait(&flags[F_X], 1u);
    const LAS unsigned* xl = (const LAS unsigned*)(L + L_X);
    const LAS u16* xh = (const LAS u16*)(L + L_X);
    unsigned xw[5];
#pragma unroll
    for (int i = 0; i < 4; ++i) xw[i] = xl[lane + 64 * i];
    xw[4] = lane < 32 ? xl[256 + lane] : 0u;
    const unsigned xq0 = xh[144 * rs + lane], xq1 = xh[144 * rs + 64 + lane], xq2 = xh[144 * rs + 128 + (lane & 15)];
    const unsigned xg0 = xh[36 * sp + (lane < 36 ? lane : 0)];
    const unsigned xr0 = xh[64 * (rfin >> 4) + 32 * ((rfin >> 3) & 1) + 8 * (lane & 3) + (rfin & 7)];
    tag = mk_tag(flags[W_TAG]);
    float xv[10];
#pragma unroll
    for (int i = 0; i < 5; ++i) { xv[2 * i] = asf(xw[i] << 16); xv[2 * i + 1] = asf(xw[i] & 0xffff0000u); }
    const float s = ((xv[0] + xv[1]) + (xv[2] + xv[3])) + ((xv[4] + xv[5]) + (xv[6] + xv[7])) + (xv[8] + xv[9]);
    const float mean = wave_sum_s(s) * (1.f / D);
    float q2 = 0.f, q3 = 0.f;
#pragma unroll
    for (int i = 0; i < 8; i += 2) { const float d0 = xv[i] - mean, d1 = xv[i + 1] - mean; q2 = fmaf(d0, d0, q2); q3 = fmaf(d1, d1, q3); }
    { const float d0 = lane < 32 ? xv[8] - mean : 0.f, d1 = lane < 32 ? xv[9] - mean : 0.f; q2 = fmaf(d0, d0, q2); q3 = fmaf(d1, d1, q3); }
    const float var = wave_sum_s(q2 + q3) * (1.f / D);
    const float mb = rbf(mean);
    const float den = rbf(__builtin_amdgcn_sqrtf(rbf(rbf(var) + 1e-5f)));
    const float rden = __builtin_amdgcn_rcpf(den);
    auto norm = [&](unsigned raw) -> unsigned {
      const float a = rbf(asf(raw << 16) - mb);
      float qv = a * rden; qv = fmaf(fmaf(-qv, den, a), rden, qv);
      return cvtpk(qv, qv);
    };
    LAS unsigned* xq = (LAS unsigned*)(L + L_XQ);
    xq[lane] = norm(xq0); xq[64 + lane] = norm(xq1);
    { const unsigned v2 = norm(xq2); if (lane < 16) xq[128 + lane] = v2; }
    WAIT_LGKM0();
    unsigned xv18[18];
    {
      const LAS unsigned* src = xq + 18 * g8;
#pragma unroll
      for (int i = 0; i < 9; ++i) { const v2u t2 = *(const LAS v2u*)(src + 2 * i); xv18[2 * i] = t2.x; xv18[2 * i + 1] = t2.y; }
    }
    if (pass) STAMP(1);
    f32x4 acc = {0.f, 0.f, 0.f, 0.f};
    if (pass) lds_wait(&flags[F_WQ], 1u);
#pragma unroll
    for (int t = 0; t < 18; ++t) acc = mfma(((const LAS v4u*)(L + L_WQ))[t * 64 + lane], sel_b(xv18[t], sel), acc);
    {
      const f32x4 y = strip_fold(acc);
      st_sc1(rws, pass ? qpoff : OOB, pack4(y, tag));
    }
    if (pass) STAMP(2);
    { const unsigned vg = norm(xg0); if (lane < 40) ((LAS unsigned*)(L + L_XG))[lane] = lane < 36 ? vg : 0u; }
    if (lane < 4) ((LAS float*)(L + L_XR))[lane] = asf(xr0 << 16);
    WAIT_LGKM0();
    if (lane == 0 && pass) flags[F_XN] = 1u;
    }
    {
      unsigned off[4]; bool live[4]; v4u g[4];
#pragma unroll
      for (int r = 0; r < 4; ++r) { off[r] = (unsigned)(WS_QP + (kvh * 4 + r) * 1024 + lane * 16); live[r] = true; }
      __builtin_amdgcn_s_sleep(DLY_Q);
      poll<4>(rws, off, live, tag, g);
      const f32x4 q4 = (asf4(g[0]) + asf4(g[1])) + (asf4(g[2]) + asf4(g[3]));
      LAS u16* qb = (LAS u16*)(L + L_QB) + 64 * (lane >> 4) + 32 * (g8 & 1) + c8;
      const unsigned p01 = cvtpk(q4[0], q4[1]), p23 = cvtpk(q4[2], q4[3]);
      qb[0] = (u16)p01; qb[8] = (u16)(p01 >> 16); qb[16] = (u16)p23; qb[24] = (u16)(p23 >> 16);
      WAIT_LGKM0();
      if (lane == 0) flags[F_Q] = 1u;
    }
    STAMP(3);
    unsigned ooff[2];
    wo_offsets(c, 8, g8, c8, ooff);
    const unsigned poff8 = strip_off(lane, 8);
    const rsrc_t ro = mk_rsrc(p.wo, NQ * D * 2);
    lds_wait_slow(&flags[F_OP], 1u);
    STAMP(4);
    v4u WO[2];
    WO[0] = ld_nt(ro, ooff[0]); WO[1] = ld_nt(ro, ooff[1]);
    PIN();
    {
      const unsigned pb = (unsigned)(WS_OP + (kvh * 16 + (lane & 15)) * 1152);
      unsigned off[2] = {pb + (4 * sp + (lane >> 4)) * 16, pb + (64 + (sp >> 3)) * 16};
      bool live[2] = {true, true}; v4u g[2];
      __builtin_amdgcn_s_sleep(DLY_O);
      poll<2>(rws, off, live, tag, g);
      const float m = asf(g[1].x | (g[1].y >> 10)), l = asf(g[1].z | (g[1].w >> 10));
      const float M = row_max16(m);
      const float wgt = __builtin_amdgcn_exp2f(m - M);
      const float Ls = row_sum16(wgt * l);
      const float o = tr_sum16(wgt * asf4(g[0]), lane) * __builtin_amdgcn_rcpf(Ls);
      if ((lane & 3) == 0) ((LAS unsigned*)(L + L_OS))[lane >> 2] = cvtpk(o, o);
      WAIT_LGKM0();
    }
    STAMP(5);
    wo_partial(c, poff8, *(const LAS v2u*)(L + L_OS + 8 * g8), WO, sel, tag);
    if (lg < 18) {
      lds_wait_slow(&flags[C_SP], 9u);
      __builtin_amdgcn_s_sleep(DLY_S);
      const float sv = final_sum(rws, (unsigned)(WS_SP + rfin * 16), lane, tag);
      if (lane < 16 && (lane & 3) == 0) ((LAS float*)(L + L_FIN))[lane >> 2] = sv;
      WAIT_LGKM0();
      if (lane == 0) flags[F_S] = 1u;
      STAMP(6);
    }
  } else {
    const Tag tag = mk_tag(flags[W_TAG]);
    int late;
    {
      wait_until(t_start + TB_READ);
      const v4u g = untag(ld_sc1(rws, lane < 8 ? (unsigned)(WS_TB + lane * 128) : OOB), tag);
      const bool ok = lane < 8 && (resid(g) & TMASK) == 0u;
      late = ok ? (int)(t_start - (g.x | (g.y >> 10))) : 0;
      late = late > TB_MAX ? 0 : late;
      late = max(late, __builtin_amdgcn_mov_dpp(late, XOR1, 0xf, 0xf, true));
      late = max(late, __builtin_amdgcn_mov_dpp(late, XOR2, 0xf, 0xf, true));
      late = max(late, __builtin_amdgcn_mov_dpp(late, XOR7, 0xf, 0xf, true));
      late = UNI(late);
      if (lane == 0 && late > 0) flags[W_SHIFT] = (unsigned)late;
    }
    asm volatile("" :: "v"(cpf0), "v"(cpf1), "v"(cpf2), "v"(tl0), "v"(tl1), "v"(tl2), "v"(tl3));
    const Sel sel = mk_sel(lane);
    const int g0 = 9 * (idx >> 1) + 4 * (idx & 1), odd = idx & 1;
    unsigned doff[3];
    wd_offsets(c, 8, g8, c8, doff);
    const unsigned poff8 = strip_off(lane, 8);
    const rsrc_t rd = mk_rsrc(p.wd, F * D * 2);
    const bool lc = odd && lane < 32;
    const unsigned pb = (unsigned)(WS_GP + (xg * 32 + (lane & 15)) * 2304);
    unsigned off[3] = {pb + (g0 + (lane >> 4)) * 16, pb + 16 * 2304 + (g0 + (lane >> 4)) * 16, lc ? pb + (lane >> 4) * (16 * 2304) + (g0 + 4) * 16 : OOB};
    bool live[3] = {true, true, lc}; v4u g[3];
    const int vi = (lane >> 2) & 3;
    const int col = 64 * (rfin >> 4) + 32 * ((rfin >> 3) & 1) + 8 * vi + (rfin & 7);
    wait_until(t_start + T_LAST - (unsigned)late);
    v4u WD[3];
#pragma unroll
    for (int t = 0; t < 3; ++t) WD[t] = ld_nt(rd, doff[t]);
    PIN();
    lds_wait(&flags[C_GU], 9u);
    STAMP(1);
    __builtin_amdgcn_s_sleep(DLY_H);
    poll<3>(rws, off, live, tag, g);
    STAMP(2);
    if (!lc) g[2] = v4u{0u, 0u, 0u, 0u};
    {
      const float gs = tr_sum16(asf4(g[0]), lane), us = tr_sum16(asf4(g[1]), lane), cs = tr_sum16(asf4(g[2]), lane);
      auto sw = __builtin_amdgcn_permlane16_swap(fbits(cs), fbits(cs), false, false);
      const bool ex = (lane & 3) == 1;
      const float gin = rbf(ex ? asf(sw[0]) : gs), uin = rbf(ex ? asf(sw[1]) : us);
      const float hv = rbf(silu_f(gin)) * uin;
      LAS unsigned* hs = (LAS unsigned*)(L + L_HS);
      if ((lane & 3) == 0 || (ex && lane < 16)) hs[ex ? 16 + vi : lane >> 2] = cvtpk(hv, hv);
    }
    WAIT_LGKM0();
    STAMP(3);
    wd_partial(c, poff8, *(const LAS v2u*)(L + L_HS + 8 * g8), ((const LAS unsigned*)(L + L_HS))[16 + (g8 & 3)], WD, sel, tag);
    if (lg < 18) {
      const float xr = ((const LAS float*)(L + L_XR))[vi];
      lds_wait(&flags[C_FP], 9u);
      STAMP(4);
      __builtin_amdgcn_s_sleep(DLY_F);
      const float fv = final_sum(rws, (unsigned)(WS_FP + rfin * 16), lane, tag);
      STAMP(5);
      const float gate = rbf(silu_f(rbf(fv)));
      lds_wait(&flags[F_S], 1u);
      const float sv = ((const LAS float*)(L + L_FIN))[vi];
      const float res = xr + rbf(rbf(sv) * gate);
      if (lane < 16 && (lane & 3) == 0) p.out[col] = (u16)cvtpk(res, res);
      if (b == 0 && lane == 0) __hip_atomic_store((GAS unsigned*)(p.ws + WS_EPOCH), flags[W_TAG], __ATOMIC_RELAXED, __HIP_MEMORY_SCOPE_AGENT);
      STAMP(6);
    }
  }
  } else {
  const int st = wave - 1;
  const Sel sel = mk_sel(lane);
  const rsrc_t rg = mk_rsrc((idx >> 4) ? p.wu : p.wg, D * F * 2);
  unsigned goff, goff4, ooff[2], doff[3];
  auto late_addr = [&]() {
    PIN();
    int ln = lane; asm volatile("" : "+v"(ln));
    const int g8_ = ln >> 3, c8_ = ln & 7;
    const unsigned gcol = (unsigned)(576 * xg + 64 * st + 8 * c8_);
    goff = (unsigned)(((36 * sp + 4 * g8_) * F + gcol) * 2);
    goff4 = g8_ < 4 ? (unsigned)(((36 * sp + 32 + g8_) * F + gcol) * 2) : OOB;
    wo_offsets(c, st, g8_, c8_, ooff);
    wd_offsets(c, st, g8_, c8_, doff);
    PIN();
  };
  const unsigned poff = strip_off(lane, st);
  v4u G[5];
  unsigned tn;
#define SLOT(dt) do { wait_until(tn); tn += (dt); } while (0)
  auto issue_g = [&](int t) { SLOT(DT2); G[t] = ld_nt(rg, t < 4 ? goff + t * (F * 2) : goff4); PIN(); };
  v4u gb[5];
  auto gu_frags = [&]() {
    const v4u x4 = *(const LAS v4u*)(L + L_XG + 16 * g8);
    const unsigned x5 = ((const LAS unsigned*)(L + L_XG))[32 + g8];
#pragma unroll
    for (int t = 0; t < 4; ++t) gb[t] = sel_b(x4[t], sel);
    gb[4] = sel_b(x5, sel);
    PIN();
  };
  auto do_gu = [&](const Tag& tag) {
    f32x4 acc = {0.f, 0.f, 0.f, 0.f};
#pragma unroll
    for (int t = 0; t < 5; ++t) acc = mfma(G[t], gb[t], acc);
    publish_at(c, poff + WS_GP + (xg * 32 + idx) * 2304, acc, tag);
    if (lane == 0) lds_add(&flags[C_GU]);
    PIN();
  };

  if (wave > 8) {
    tn = now32() + TSTART + 2 * DT1;
    late_addr();
#pragma unroll
    for (int t = 0; t < 5; ++t) { issue_g(t); tn += W9GAP - DT2; }
    lds_wait_slow(&flags[F_XN], 1u);
    const Tag tag = mk_tag(flags[W_TAG]);
    gu_frags();
    do_gu(tag);
    STAMP(2);
    lds_wait(&flags[C_ATT], 8u);
    STAMP(3);
    const int hh = g8 & 1;
    f32x2 ml[8]; f32x4 po[8];
#pragma unroll
    for (int w = 0; w < 8; ++w) { ml[w] = ((const LAS f32x2*)(L + L_ML))[w * 2 + hh]; po[w] = ((const LAS f32x4*)(L + L_PO))[w * 64 + lane]; }
    const float M = fmaxf(fmaxf(fmaxf(ml[0].x, ml[1].x), fmaxf(ml[2].x, ml[3].x)), fmaxf(fmaxf(ml[4].x, ml[5].x), fmaxf(ml[6].x, ml[7].x)));
    f32x4 o = {0.f, 0.f, 0.f, 0.f}; float Ls = 0.f;
#pragma unroll
    for (int w = 0; w < 8; ++w) {
      const float sc = __builtin_amdgcn_exp2f(ml[w].x - M);
      Ls = fmaf(sc, ml[w].y, Ls);
      o += sc * po[w];
    }
    const unsigned pbase = (unsigned)(WS_OP + (kvh * 16 + sp) * 1152);
    st_sc1(rws, pbase + (hh * 32 + (lane >> 4) * 8 + c8) * 16, pack4(o, tag));
    if ((lane & 0x37) == 0) st_sc1(rws, pbase + (64 + hh) * 16, pack2x(fbits(M), fbits(Ls), tag));
    if (lane == 0) flags[F_OP] = 1u;
    PIN();
    STAMP(4);
  } else {
    const size_t tile = ((size_t)kvh * SK + (SK - WIN)) * DH;
    const rsrc_t rk = mk_rsrc(p.kc + tile, WIN * DH * 2);
    const rsrc_t rv = mk_rsrc(p.vc + tile, WIN * DH * 2);
    const int cbase = (st * 8 * 16 + sp) * 1024;
    const unsigned koff = (unsigned)(((lane >> 2) & 3) * 256 + ((lane & 3) + 4 * (lane >> 4)) * 16);
    STAMP(13);
    tn = now32() + TSTART + (st * DT1) / 8;
    v4u K[8], V[8];
#define LDK(t) do { SLOT(DT1); if (t >= KWIN) WAIT_VM(KWIN - 1); K[t] = __builtin_amdgcn_raw_buffer_load_b128(rk, (int)koff, cbase + t * 16384, AUX_NT); PIN(); } while (0)
#define LDV(t) do { SLOT(DT1); WAIT_VM(KWIN - 1); V[t] = __builtin_amdgcn_raw_buffer_load_b128(rv, lane * 16, cbase + t * 16384, AUX_NT); PIN(); } while (0)
    unsigned shifted = 0;
#define CATCH_UP() do { const unsigned sh_ = UNI(flags[W_SHIFT]); tn -= sh_ - shifted; shifted = sh_; } while (0)
#pragma unroll
    for (int t = 0; t < 8; ++t) { LDK(t); if (t == 1) late_addr(); if (t == 5) CATCH_UP(); }
    constexpr float C2 = 0.08838834764831845f * 1.4426950408889634f;
    unsigned pk[8];
    auto qk_softmax = [&]() {
      const v4u qf = *(const LAS v4u*)(L + L_QB + ((g8 & 1) * 128 + ((lane & 3) + 4 * (lane >> 4)) * 8) * 2);
      float s[8];
#pragma unroll
      for (int t = 0; t < 8; ++t) {
        const f32x4 d = mfma(K[t], qf, f32x4{0.f, 0.f, 0.f, 0.f});
        s[t] = (dpp<0x00>(d[0]) + dpp<0x55>(d[1])) + (dpp<0xAA>(d[2]) + dpp<0xFF>(d[3]));
      }
      float m = fmaxf(fmaxf(fmaxf(s[0], s[1]), fmaxf(s[2], s[3])), fmaxf(fmaxf(s[4], s[5]), fmaxf(s[6], s[7])));
      m = x16_max(m) * C2;
      float l = 0.f;
#pragma unroll
      for (int t = 0; t < 8; ++t) { const float pr = __builtin_amdgcn_exp2f(fmaf(s[t], C2, -m)); l += pr; pk[t] = cvtpk(pr, pr); }
      l = x16_sum(l);
      if ((lane & 0x37) == 0) ((LAS f32x2*)(L + L_ML))[st * 2 + g8] = f32x2{m, l};
      PIN();
    };
#pragma unroll
    for (int t = 0; t < QKPOS; ++t) { LDV(t); if (t == 0 || t == 3) CATCH_UP(); }
    const bool early = UNI(flags[F_Q]) != 0u;
    if (early) qk_softmax();
    STAMP(3);
#pragma unroll
    for (int t = QKPOS; t < 8; ++t) LDV(t);
    STAMP(11);
    tn -= (st * DT1) / 8;
#pragma unroll
    for (int t = 0; t < QKG; ++t) issue_g(t);
    STAMP(1);
    if (!early) { lds_wait(&flags[F_Q], 1u); qk_softmax(); }
    STAMP(2);
    f32x4 oacc = {0.f, 0.f, 0.f, 0.f};
#pragma unroll
    for (int t = 0; t < 8; ++t) oacc = mfma(V[t], sel_b(pk[t], sel), oacc);
    ((LAS f32x4*)(L + L_PO))[st * 64 + lane] = oacc;
    WAIT_LGKM0();
    if (lane == 0) lds_add(&flags[C_ATT]);
    PIN();
    STAMP(4);
#pragma unroll
    for (int t = QKG; t < 5; ++t) issue_g(t);
    const Tag tag = mk_tag(flags[W_TAG]);
    gu_frags();
    const rsrc_t ro = mk_rsrc(p.wo, NQ * D * 2);
    const rsrc_t rd = mk_rsrc(p.wd, F * D * 2);
    v4u WO[2], WD[3];
    f32x4 gacc = {0.f, 0.f, 0.f, 0.f};
#pragma unroll
    for (int t = 0; t < 3; ++t) gacc = mfma(G[t], gb[t], gacc);
    PIN();
    tn += DTD - DT2;
    SLOT(DTD); WD[0] = ld_nt(rd, doff[0]); PIN();
    STAMP(5);
    gacc = mfma(G[3], gb[3], gacc);
    gacc = mfma(G[4], gb[4], gacc);
    publish_at(c, poff + WS_GP + (xg * 32 + idx) * 2304, gacc, tag);
    if (lane == 0) lds_add(&flags[C_GU]);
    PIN();
    STAMP(6);
    SLOT(DTD); WD[1] = ld_nt(rd, doff[1]); PIN();
    if (idx & 1) SLOT(DTD);
    WD[2] = ld_nt(rd, doff[2]); PIN();
    tn += DTO - DTD;
    SLOT(DT2); WO[0] = ld_nt(ro, ooff[0]); PIN();
    SLOT(DT2); WO[1] = ld_nt(ro, ooff[1]); PIN();
    v2u h2, o2; unsigned h3, spins = 0; bool hh;
#pragma nounroll
    for (;;) {
      h2 = *(const volatile LAS v2u*)(L + L_HS + 8 * g8); h3 = ((const volatile LAS unsigned*)(L + L_HS))[16 + (g8 & 3)];
      o2 = *(const volatile LAS v2u*)(L + L_OS + 8 * g8);
      hh = __all(h2.x != EMPTY && h2.y != EMPTY && h3 != EMPTY);
      if (hh || __all(o2.x != EMPTY && o2.y != EMPTY) || ++spins > (SPIN_CAP << 6)) break;
    }
    if (hh) {
      wd_partial(c, poff, h2, h3, WD, sel, tag);
      STAMP(8);
#pragma nounroll
      while (!__all(o2.x != EMPTY && o2.y != EMPTY) && ++spins <= (SPIN_CAP << 6)) o2 = *(const volatile LAS v2u*)(L + L_OS + 8 * g8);
      wo_partial(c, poff, o2, WO, sel, tag);
      STAMP(7);
    } else {
      wo_partial(c, poff, o2, WO, sel, tag);
      STAMP(7);
#pragma nounroll
      while (!__all(h2.x != EMPTY && h2.y != EMPTY && h3 != EMPTY) && ++spins <= (SPIN_CAP << 6)) {
        h2 = *(const volatile LAS v2u*)(L + L_HS + 8 * g8); h3 = ((const volatile LAS unsigned*)(L + L_HS))[16 + (g8 & 3)];
      }
      wd_partial(c, poff, h2, h3, WD, sel, tag);
      STAMP(8);
    }
  }
  }
  STAMP_FLUSH();
}

}

extern "C" void kernel_launch(void* const* d_in, const int* in_sizes, int n_in, void* d_out, int out_size, void* d_ws, size_t ws_size, hipStream_t stream) {
  Params p;
  p.x = (const u16*)d_in[0]; p.kc = (const u16*)d_in[1]; p.vc = (const u16*)d_in[2];
  p.wq = (const u16*)d_in[3]; p.wo = (const u16*)d_in[4];
  p.wg = (const u16*)d_in[6]; p.wu = (const u16*)d_in[7]; p.wd = (const u16*)d_in[8];
  p.out = (u16*)d_out; p.ws = (unsigned char*)d_ws;
  (void)hipFuncSetAttribute((const void*)fused_decode, hipFuncAttributeMaxDynamicSharedMemorySize, LDS_BYTES);
  hipLaunchKernelGGL(fused_decode, dim3(NWG), dim3(NTHR), LDS_BYTES, stream, p);
}
```

```cpp
#include <hip/hip_runtime.h>
#include <hip/hip_bf16.h>
#include <stdint.h>

#define GAS __attribute__((address_space(1)))
#define LAS __attribute__((address_space(3)))
typedef unsigned v4u __attribute__((ext_vector_type(4)));
typedef unsigned v2u __attribute__((ext_vector_type(2)));
typedef float f32x4 __attribute__((ext_vector_type(4)));
typedef float f32x2 __attribute__((ext_vector_type(2)));
typedef short bf16x8 __attribute__((ext_vector_type(8)));
typedef __bf16 bf16x2 __attribute__((ext_vector_type(2)));
typedef unsigned short u16;
typedef __amdgpu_buffer_rsrc_t rsrc_t;

#define KWIN 4
#define DT1 34
#define DT2 36
#define DTD 36
#define DTO 36
#define DRY0 0
#define OUTP 1
#define TLBP 0
#define TSTART 10
#define QKPOS 8
#define W9GAP 70
#define T_LAST 790
#define CODE_BYTES 18176
#define CODE_PF_WGS 2
#define TB_READ 170
#define TB_MAX 300
#define DLY_Q 36
#define DLY_O 30
#define DLY_H 20
#define DLY_F 20
#define DLY_S 28
#define RETRY 8

namespace {

constexpr int D = 576, SK = 65536, DH = 128, WIN = 4096, F = 4608, NQ = 4096;
constexpr int NWG = 256, NWAVE = 11, NTHR = NWAVE * 64;
constexpr int LDS_BYTES = 84 * 1024;

constexpr int WS_EPOCH = 0;
constexpr int WS_TB = 256;
constexpr int WS_QP = WS_TB + 1024;
constexpr int WS_OP = WS_QP + 16 * 4096;
constexpr int WS_GP = WS_OP + 16 * 16 * 1152;
constexpr int WS_SP = WS_GP + 8 * 32 * 2304;
constexpr int WS_FP = WS_SP + 256 * 2304;
constexpr int WS_END = WS_FP + 256 * 2304;

constexpr int L_FLAG = 0;
constexpr int L_OS = 64;
constexpr int L_HS = L_OS + 64;
constexpr int L_XQ = L_HS + 128;
constexpr int L_XG = L_XQ + 576;
constexpr int L_XR = L_XG + 160;
constexpr int L_QB = L_XR + 16;
constexpr int L_ML = L_QB + 512;
constexpr int L_FIN = L_ML + 128;
constexpr int L_PO = L_FIN + 16;
constexpr int L_X = L_PO + 8192;
constexpr int L_WQ = L_X + 2048;
constexpr int L_TS = L_WQ + 18 * 1024;
static_assert(L_PO % 16 == 0 && L_XQ % 16 == 0 && L_XG % 16 == 0 && L_QB % 16 == 0 && L_OS % 16 == 0 && L_HS % 16 == 0 && L_XR % 16 == 0 && L_X % 16 == 0, "align");
enum { F_XN = 0, F_Q = 1, C_ATT = 2, C_GU = 3, F_OP = 4, C_SP = 5, C_FP = 6, F_S = 7, W_TAG = 8, W_SHIFT = 9, F_X = 10, F_WQ = 11 };

constexpr int AUX_NT = 2, AUX_SC1 = 16;
constexpr unsigned VMASK = 0xFFFFFC00u, TMASK = 0x3FFu, OOB = 0xFFFFFF00u;
constexpr unsigned EMPTY = 0xFFFF0000u;
constexpr unsigned SPIN_CAP = 1u << 17;

struct Params {
  const u16* x; const u16* kc; const u16* vc; const u16* wq; const u16* wo;
  const u16* wg; const u16* wu; const u16* wd; u16* out; unsigned char* ws;
};

__device__ __forceinline__ unsigned fbits(float f) { return __builtin_bit_cast(unsigned, f); }
__device__ __forceinline__ float asf(unsigned u) { return __builtin_bit_cast(float, u); }
__device__ __forceinline__ unsigned cvtpk(float a, float b) { bf16x2 r = {(__bf16)a, (__bf16)b}; return __builtin_bit_cast(unsigned, r); }
__device__ __forceinline__ float rbf(float f) { return asf(cvtpk(0.f, f) & 0xffff0000u); }
__device__ __forceinline__ float silu_f(float x) { return x * __builtin_amdgcn_rcpf(1.f + __builtin_amdgcn_exp2f(x * -1.4426950408889634f)); }
__device__ __forceinline__ f32x4 mfma(v4u a, v4u b, f32x4 c) {
  return __builtin_amdgcn_mfma_f32_16x16x32_bf16(__builtin_bit_cast(bf16x8, a), __builtin_bit_cast(bf16x8, b), c, 0, 0, 0);
}
template <int CTRL> __device__ __forceinline__ float dpp(float x) {
  return asf(__builtin_amdgcn_mov_dpp(__builtin_bit_cast(int, x), CTRL, 0xf, 0xf, true));
}
template <int CTRL, int RM> __device__ __forceinline__ float dppm(float x) {
  return asf(__builtin_amdgcn_update_dpp(0, __builtin_bit_cast(int, x), CTRL, RM, 0xf, true));
}
constexpr int XOR1 = 0xB1, XOR2 = 0x4E, XOR7 = 0x141, ROR8 = 0x128;
__device__ __forceinline__ float x16_max(float x) {
  auto s = __builtin_amdgcn_permlane16_swap(fbits(x), fbits(x), false, false);
  x = fmaxf(asf(s[0]), asf(s[1]));
  auto t = __builtin_amdgcn_permlane32_swap(fbits(x), fbits(x), false, false);
  return fmaxf(asf(t[0]), asf(t[1]));
}
__device__ __forceinline__ float x16_sum(float x) {
  auto s = __builtin_amdgcn_permlane16_swap(fbits(x), fbits(x), false, false);
  x = asf(s[0]) + asf(s[1]);
  auto t = __builtin_amdgcn_permlane32_swap(fbits(x), fbits(x), false, false);
  return asf(t[0]) + asf(t[1]);
}
__device__ __forceinline__ float row_sum16(float x) { x += dpp<XOR1>(x); x += dpp<XOR2>(x); x += dpp<XOR7>(x); x += dpp<ROR8>(x); return x; }
__device__ __forceinline__ float row_max16(float x) { x = fmaxf(x, dpp<XOR1>(x)); x = fmaxf(x, dpp<XOR2>(x)); x = fmaxf(x, dpp<XOR7>(x)); x = fmaxf(x, dpp<ROR8>(x)); return x; }
__device__ __forceinline__ float wave_sum_s(float s) {
  s += dpp<0x111>(s); s += dpp<0x112>(s); s += dpp<0x114>(s); s += dpp<0x118>(s);
  s += dppm<0x142, 0xa>(s); s += dppm<0x143, 0xc>(s);
  return asf(__builtin_amdgcn_readlane(fbits(s), 63));
}
__device__ __forceinline__ float tr_sum16(f32x4 v, int lane) {
  const bool b3 = lane & 8, b2 = lane & 4;
  const float a = (b3 ? v[2] : v[0]) + dpp<ROR8>(b3 ? v[0] : v[2]);
  const float b = (b3 ? v[3] : v[1]) + dpp<ROR8>(b3 ? v[1] : v[3]);
  float r = (b2 ? b : a) + dpp<XOR7>(b2 ? a : b);
  r += dpp<XOR2>(r); r += dpp<XOR1>(r);
  return r;
}

__device__ __forceinline__ rsrc_t mk_rsrc(const void* p, unsigned bytes) {
  return __builtin_amdgcn_make_buffer_rsrc((void*)p, 0, (int)bytes, 0x00020000);
}
__device__ __forceinline__ v4u ld_nt(rsrc_t r, unsigned off) { return __builtin_amdgcn_raw_buffer_load_b128(r, (int)off, 0, AUX_NT); }
__device__ __forceinline__ v4u ld_sc1(rsrc_t r, unsigned off) { return __builtin_amdgcn_raw_buffer_load_b128(r, (int)off, 0, AUX_SC1); }
__device__ __forceinline__ void st_sc1(rsrc_t r, unsigned off, v4u g) { __builtin_amdgcn_raw_buffer_store_b128(g, r, (int)off, 0, AUX_SC1); }
__device__ __forceinline__ unsigned now32() { unsigned long long t; asm volatile("s_memrealtime %0\n\ts_waitcnt lgkmcnt(0)" : "=s"(t) :: "memory"); return (unsigned)t; }
__device__ __forceinline__ void wait_until(unsigned t) {
#pragma nounroll
  while ((int)(now32() - t) < 0) __builtin_amdgcn_s_sleep(1);
}
#define PIN() __builtin_amdgcn_sched_barrier(0)
#define CBAR() asm volatile("" ::: "memory")
#define WAIT_VM(n) asm volatile("s_waitcnt vmcnt(%0)" ::"i"(n) : "memory")
#define WAIT_LGKM0() asm volatile("s_waitcnt lgkmcnt(0)" ::: "memory")
#define UNI(x) __builtin_amdgcn_readfirstlane(x)

struct Tag { unsigned t0, t1, t2, t3; };
__device__ __forceinline__ Tag mk_tag(unsigned T) { return Tag{T & TMASK, (T + 0x155u) & TMASK, (T + 0x2AAu) & TMASK, (T + 0x0FFu) & TMASK}; }
__device__ __forceinline__ v4u pack4(f32x4 v, const Tag& t) {
  return v4u{(fbits(v.x) & VMASK) | t.t0, (fbits(v.y) & VMASK) | t.t1, (fbits(v.z) & VMASK) | t.t2, (fbits(v.w) & VMASK) | t.t3};
}
__device__ __forceinline__ v4u pack2x(unsigned a, unsigned b, const Tag& t) {
  return v4u{(a & VMASK) | t.t0, ((a & TMASK) << 10) | t.t1, (b & VMASK) | t.t2, ((b & TMASK) << 10) | t.t3};
}
__device__ __forceinline__ v4u untag(v4u g, const Tag& t) { return v4u{g.x ^ t.t0, g.y ^ t.t1, g.z ^ t.t2, g.w ^ t.t3}; }
__device__ __forceinline__ unsigned resid(v4u u) { return (u.x | u.y | u.z) | u.w; }
__device__ __forceinline__ f32x4 asf4(v4u u) { return __builtin_bit_cast(f32x4, u); }

template <int N>
__device__ __forceinline__ void poll(rsrc_t r, const unsigned (&off)[N], const bool (&live)[N], const Tag& tag, v4u (&res)[N]) {
  unsigned spins = 0;
#pragma nounroll
  for (;;) {
    v4u a[N];
#pragma unroll
    for (int i = 0; i < N; ++i) a[i] = ld_sc1(r, off[i]);
    CBAR();
    unsigned bad = 0;
#pragma unroll
    for (int i = 0; i < N; ++i) { res[i] = untag(a[i], tag); bad |= live[i] ? resid(res[i]) : 0u; }
    if (__all((bad & TMASK) == 0u) || ++spins > SPIN_CAP) break;
    __builtin_amdgcn_s_sleep(RETRY);
  }
}
__device__ __forceinline__ float final_sum(rsrc_t rws, unsigned base, int lane, const Tag& tag) {
  unsigned off[4]; bool live[4]; v4u g[4];
#pragma unroll
  for (int i = 0; i < 4; ++i) { off[i] = base + (unsigned)((i * 64 + lane) * 2304); live[i] = true; }
  poll<4>(rws, off, live, tag, g);
  const f32x4 v = (asf4(g[0]) + asf4(g[1])) + (asf4(g[2]) + asf4(g[3]));
  return x16_sum(tr_sum16(v, lane));
}

__device__ __forceinline__ void lds_wait(volatile LAS unsigned* f, unsigned want) {
  unsigned spins = 0;
#pragma nounroll
  while (*f < want) { if (++spins > (SPIN_CAP << 6)) break; }
  CBAR();
}
__device__ __forceinline__ void lds_wait_slow(volatile LAS unsigned* f, unsigned want) {
  unsigned spins = 0;
#pragma nounroll
  while (*f < want) { __builtin_amdgcn_s_sleep(2); if (++spins > (SPIN_CAP << 4)) break; }
  CBAR();
}
__device__ __forceinline__ void lds_add(volatile LAS unsigned* f) { __hip_atomic_fetch_add((LAS unsigned*)f, 1u, __ATOMIC_RELAXED, __HIP_MEMORY_SCOPE_WORKGROUP); }

struct Sel { unsigned m0, m1, m2, m3; };
__device__ __forceinline__ Sel mk_sel(int lane) {
  const unsigned h = (lane & 1) ? 0xFFFF0000u : 0x0000FFFFu; const int r = (lane & 7) >> 1;
  return Sel{r == 0 ? h : 0u, r == 1 ? h : 0u, r == 2 ? h : 0u, r == 3 ? h : 0u};
}
__device__ __forceinline__ v4u sel_b(unsigned dup, const Sel& s) { return v4u{dup & s.m0, dup & s.m1, dup & s.m2, dup & s.m3}; }
__device__ __forceinline__ f32x4 strip_fold(f32x4 d) {
  f32x4 y;
#pragma unroll
  for (int i = 0; i < 4; ++i) {
    const float t = dpp<ROR8>(d[i]);
    auto s = __builtin_amdgcn_permlane32_swap(fbits(t), fbits(t), false, false);
    y[i] = d[i] + asf(s[1]);
  }
  return y;
}

struct Ctx {
  LAS unsigned char* L; volatile LAS unsigned* flags; rsrc_t rws;
  int lane, g8, c8, b, xg, idx, kvh, sp;
};
__device__ __forceinline__ void wo_offsets(const Ctx& c, int st, int g8, int c8, unsigned (&off)[2]) {
  const int hrow = (2 * c.kvh + (c.sp >> 3)) * 128 + 32 * ((c.sp >> 1) & 3) + 4 * (c.sp & 1);
#pragma unroll
  for (int t = 0; t < 2; ++t) { const int u = 2 * g8 + t; off[t] = (unsigned)(((hrow + 8 * (u & 3) + (u >> 2)) * D + 64 * st + 8 * c8) * 2); }
}
__device__ __forceinline__ void wd_offsets(const Ctx& c, int st, int g8, int c8, unsigned (&off)[3]) {
  const int odd = c.idx & 1, g0 = 9 * (c.idx >> 1) + 4 * odd;
#pragma unroll
  for (int t = 0; t < 3; ++t) {
    const int u = t < 2 ? 2 * g8 + t : 16 + (g8 & 3), Gi = g0 + (u >> 2);
    const int row = 576 * c.xg + 64 * (Gi >> 4) + 32 * ((Gi >> 3) & 1) + 8 * (u & 3) + (Gi & 7);
    off[t] = (t < 2 || (odd && g8 < 4)) ? (unsigned)((row * D + 64 * st + 8 * c8) * 2) : OOB;
  }
}
__device__ __forceinline__ unsigned strip_off(int lane, int st) {
  unsigned o = (lane < 32 && (lane & 15) < 8) ? (unsigned)((st * 16 + (lane >> 4) * 8 + (lane & 7)) * 16) : 0x80000000u;
  asm volatile("" : "+v"(o));
  return o;
}
__device__ __forceinline__ void publish_at(const Ctx& c, unsigned off, f32x4 acc, const Tag& tag) {
  st_sc1(c.rws, off, pack4(strip_fold(acc), tag));
}
__device__ __forceinline__ void wo_partial(const Ctx& c, unsigned poff, v2u o2, const v4u (&WO)[2], const Sel& sel, const Tag& tag) {
  f32x4 acc = {0.f, 0.f, 0.f, 0.f};
  acc = mfma(WO[0], sel_b(o2.x, sel), acc);
  acc = mfma(WO[1], sel_b(o2.y, sel), acc);
  publish_at(c, poff + WS_SP + c.b * 2304, acc, tag);
  if (c.lane == 0) lds_add(&c.flags[C_SP]);
  PIN();
}
__device__ __forceinline__ void wd_partial(const Ctx& c, unsigned poff, v2u h2, unsigned h3, const v4u (&WD)[3], const Sel& sel, const Tag& tag) {
  f32x4 acc = {0.f, 0.f, 0.f, 0.f};
  acc = mfma(WD[0], sel_b(h2.x, sel), acc);
  acc = mfma(WD[1], sel_b(h2.y, sel), acc);
  acc = mfma(WD[2], sel_b(c.g8 < 4 ? h3 : 0u, sel), acc);
  publish_at(c, poff + WS_FP + c.b * 2304, acc, tag);
  if (c.lane == 0) lds_add(&c.flags[C_FP]);
  PIN();
}

#define STAMP(i) do {} while (0)
#define STAMP_INIT() do {} while (0)
#define STAMP_FLUSH() do {} while (0)

__global__ void __launch_bounds__(NTHR, 1) fused_decode(Params p) {
  extern __shared__ __attribute__((aligned(16))) unsigned char smem[];
  LAS unsigned char* L = (LAS unsigned char*)smem;
  volatile LAS unsigned* flags = (volatile LAS unsigned*)(L + L_FLAG);
  const int lane = threadIdx.x & 63;
  const int wave = UNI(threadIdx.x >> 6);
  const int b = blockIdx.x, xg = b & 7, idx = b >> 3;
  const int kvh = 2 * xg + (idx >> 4), sp = idx & 15;
  const rsrc_t rws = mk_rsrc(p.ws, WS_END);
  const int lg = (idx >> 3) * 8 + xg;
  const int rfin = lg * 8 + (idx & 7);
  const int g8 = lane >> 3, c8 = lane & 7;
  const Ctx c{L, flags, rws, lane, g8, c8, b, xg, idx, kvh, sp};

  unsigned long long code_base;
  asm volatile("s_getpc_b64 %0" : "=s"(code_base));
  code_base &= ~255ull;
  unsigned cpf0 = 0, cpf1 = 0, cpf2 = 0;
  if ((threadIdx.x >> 6) == 10 && (int)(blockIdx.x >> 3) < CODE_PF_WGS) {
    const GAS unsigned* cp = (const GAS unsigned*)code_base + (threadIdx.x & 63) * 32;
    if ((threadIdx.x & 63) * 128 + 16384 < CODE_BYTES) cpf2 = cp[4096];
    if ((threadIdx.x & 63) * 128 + 8192 < CODE_BYTES) cpf1 = cp[2048];
    cpf0 = cp[0];
  }
  PIN();
  STAMP_INIT();
  STAMP(0);
  const unsigned t_start = now32();
  asm volatile("" :: "s"(p.x), "s"(p.kc), "s"(p.vc), "s"(p.wq), "s"(p.wo), "s"(p.wg), "s"(p.wu), "s"(p.wd), "s"(p.out), "s"(p.ws));
  if (wave == 0 && lane < 52) flags[lane] = lane < 16 ? 0u : EMPTY;
  WAIT_LGKM0();
  __builtin_amdgcn_s_barrier();
  PIN();
  STAMP(12);

  unsigned tl0 = 0, tl1 = 0, tl2 = 0, tl3 = 0;
  if (wave == 10) {
    const unsigned E = __hip_atomic_load((const GAS unsigned*)(p.ws + WS_EPOCH), __ATOMIC_RELAXED, __HIP_MEMORY_SCOPE_AGENT);
    const rsrc_t rx = mk_rsrc(p.x, D * 2);
    __builtin_amdgcn_raw_ptr_buffer_load_lds(rx, (LAS void*)(L + L_X), 16, lane * 16, 0, 0, 0);
    __builtin_amdgcn_raw_ptr_buffer_load_lds(rx, (LAS void*)(L + L_X + 1024), 16, 1024 + lane * 16, 0, 0, 0);
    const rsrc_t rq = mk_rsrc(p.wq, D * NQ * 2);
    const unsigned qoff = (unsigned)(((144 * (sp >> 2) + 18 * g8) * NQ + kvh * 256 + 64 * (sp & 3) + 8 * c8) * 2);
#pragma unroll
    for (int t = 0; t < 18; ++t) __builtin_amdgcn_raw_ptr_buffer_load_lds(rq, (LAS void*)(L + L_WQ + 1024 * t), 16, qoff + t * (NQ * 2), 0, 0, AUX_NT);
    PIN();
    {
      const rsrc_t rg = mk_rsrc((idx >> 4) ? p.wu : p.wg, D * F * 2);
      tl0 = __builtin_amdgcn_raw_buffer_load_b32(rg, (TLBP & 1) && lane < 36 ? ((36 * sp + lane) * F + 576 * xg) * 2 : OOB, 0, AUX_NT);
      const int odd = idx & 1, Gi = 9 * (idx >> 1) + 4 * odd + (lane >> 2);
      const int drow = 576 * xg + 64 * (Gi >> 4) + 32 * ((Gi >> 3) & 1) + 8 * (lane & 3) + (Gi & 7);
      tl1 = __builtin_amdgcn_raw_buffer_load_b32(mk_rsrc(p.wd, F * D * 2), (TLBP & 2) && lane < 16 + 4 * odd ? drow * D * 2 : OOB, 0, AUX_NT);
      const int orow = (2 * kvh + (sp >> 3)) * 128 + 32 * ((sp >> 1) & 3) + 4 * (sp & 1) + 8 * (lane & 3) + ((lane >> 2) & 3);
      tl2 = __builtin_amdgcn_raw_buffer_load_b32(mk_rsrc(p.wo, NQ * D * 2), (TLBP & 4) && lane < 16 ? orow * D * 2 : OOB, 0, AUX_NT);
      tl3 = __builtin_amdgcn_raw_buffer_load_b32(mk_rsrc(p.vc + ((size_t)kvh * SK + (SK - WIN)) * DH, WIN * DH * 2), (TLBP & 8) ? (lane * 16 + sp) * 1024 : OOB, 0, AUX_NT);
    }
    __builtin_amdgcn_raw_buffer_store_b16((unsigned short)0, mk_rsrc(p.out, D * 2),
                                          (OUTP && lg < 18 && lane < 4) ? (64 * (rfin >> 4) + 32 * ((rfin >> 3) & 1) + 8 * lane + (rfin & 7)) * 2 : (int)OOB, 0, 0);
    PIN();
    STAMP(13);
    __builtin_amdgcn_s_waitcnt(0x4F77);
    const unsigned T = UNI(E) + 1u;
    asm volatile("ds_write_b32 %0, %1 offset:%2\n\ts_waitcnt lgkmcnt(0)\n\tds_write_b32 %0, %3 offset:%4" :: "v"(0u), "v"(T), "i"(L_FLAG + 4 * W_TAG), "v"(1u), "i"(L_FLAG + 4 * F_X) : "memory");
    st_sc1(rws, (idx == 0 && lane == 0) ? (unsigned)(WS_TB + xg * 128) : OOB, pack2x(t_start, 0u, mk_tag(T)));
    __builtin_amdgcn_s_waitcnt(0x0F76);
    asm volatile("ds_write_b32 %0, %1 offset:%2" :: "v"(0u), "v"(1u), "i"(L_FLAG + 4 * F_WQ) : "memory");
    STAMP(14);
  }
  unsigned wave2 = wave;
  asm volatile("" : "+s"(wave2));

  if (wave2 - 1u >= 9u) {
  if (wave2 == 0) {
    const int st = sp & 3, rs = sp >> 2;
    const Sel sel = mk_sel(lane);
    const unsigned qpoff = (lane < 32 && (lane & 15) < 8) ? (unsigned)(WS_QP + ((kvh * 4 + rs) * 4 + st) * 256 + ((lane >> 4) * 8 + c8) * 16) : OOB;
    Tag tag;
    int npass = DRY0 ? 0 : 1; asm volatile("" : "+s"(npass));
#pragma nounroll
    for (int pass = npass; pass < 2; ++pass) {
    if (pass) lds_wait(&flags[F_X], 1u);
    const LAS unsigned* xl = (const LAS unsigned*)(L + L_X);
    const LAS u16* xh = (const LAS u16*)(L + L_X);
    unsigned xw[5];
#pragma unroll
    for (int i = 0; i < 4; ++i) xw[i] = xl[lane + 64 * i];
    xw[4] = lane < 32 ? xl[256 + lane] : 0u;
    const unsigned xq0 = xh[144 * rs + lane], xq1 = xh[144 * rs + 64 + lane], xq2 = xh[144 * rs + 128 + (lane & 15)];
    const unsigned xg0 = xh[36 * sp + (lane < 36 ? lane : 0)];
    const unsigned xr0 = xh[64 * (rfin >> 4) + 32 * ((rfin >> 3) & 1) + 8 * (lane & 3) + (rfin & 7)];
    tag = mk_tag(flags[W_TAG]);
    float xv[10];
#pragma unroll
    for (int i = 0; i < 5; ++i) { xv[2 * i] = asf(xw[i] << 16); xv[2 * i + 1] = asf(xw[i] & 0xffff0000u); }
    const float s = ((xv[0] + xv[1]) + (xv[2] + xv[3])) + ((xv[4] + xv[5]) + (xv[6] + xv[7])) + (xv[8] + xv[9]);
    const float mean = wave_sum_s(s) * (1.f / D);
    float q2 = 0.f, q3 = 0.f;
#pragma unroll
    for (int i = 0; i < 8; i += 2) { const float d0 = xv[i] - mean, d1 = xv[i + 1] - mean; q2 = fmaf(d0, d0, q2); q3 = fmaf(d1, d1, q3); }
    { const float d0 = lane < 32 ? xv[8] - mean : 0.f, d1 = lane < 32 ? xv[9] - mean : 0.f; q2 = fmaf(d0, d0, q2); q3 = fmaf(d1, d1, q3); }
    const float var = wave_sum_s(q2 + q3) * (1.f / D);
    const float mb = rbf(mean);
    const float den = rbf(__builtin_amdgcn_sqrtf(rbf(rbf(var) + 1e-5f)));
    const float rden = __builtin_amdgcn_rcpf(den);
    auto norm = [&](unsigned raw) -> unsigned {
      const float a = rbf(asf(raw << 16) - mb);
      float qv = a * rden; qv = fmaf(fmaf(-qv, den, a), rden, qv);
      return cvtpk(qv, qv);
    };
    LAS unsigned* xq = (LAS unsigned*)(L + L_XQ);
    xq[lane] = norm(xq0); xq[64 + lane] = norm(xq1);
    { const unsigned v2 = norm(xq2); if (lane < 16) xq[128 + lane] = v2; }
    WAIT_LGKM0();
    unsigned xv18[18];
    {
      const LAS unsigned* src = xq + 18 * g8;
#pragma unroll
      for (int i = 0; i < 9; ++i) { const v2u t2 = *(const LAS v2u*)(src + 2 * i); xv18[2 * i] = t2.x; xv18[2 * i + 1] = t2.y; }
    }
    if (pass) STAMP(1);
    f32x4 acc = {0.f, 0.f, 0.f, 0.f};
    if (pass) lds_wait(&flags[F_WQ], 1u);
#pragma unroll
    for (int t = 0; t < 18; ++t) acc = mfma(((const LAS v4u*)(L + L_WQ))[t * 64 + lane], sel_b(xv18[t], sel), acc);
    {
      const f32x4 y = strip_fold(acc);
      st_sc1(rws, pass ? qpoff : OOB, pack4(y, tag));
    }
    if (pass) STAMP(2);
    { const unsigned vg = norm(xg0); if (lane < 40) ((LAS unsigned*)(L + L_XG))[lane] = lane < 36 ? vg : 0u; }
    if (lane < 4) ((LAS float*)(L + L_XR))[lane] = asf(xr0 << 16);
    WAIT_LGKM0();
    if (lane == 0 && pass) flags[F_XN] = 1u;
    }
    {
      unsigned off[4]; bool live[4]; v4u g[4];
#pragma unroll
      for (int r = 0; r < 4; ++r) { off[r] = (unsigned)(WS_QP + (kvh * 4 + r) * 1024 + lane * 16); live[r] = true; }
      __builtin_amdgcn_s_sleep(DLY_Q);
      poll<4>(rws, off, live, tag, g);
      const f32x4 q4 = (asf4(g[0]) + asf4(g[1])) + (asf4(g[2]) + asf4(g[3]));
      LAS u16* qb = (LAS u16*)(L + L_QB) + 64 * (lane >> 4) + 32 * (g8 & 1) + c8;
      const unsigned p01 = cvtpk(q4[0], q4[1]), p23 = cvtpk(q4[2], q4[3]);
      qb[0] = (u16)p01; qb[8] = (u16)(p01 >> 16); qb[16] = (u16)p23; qb[24] = (u16)(p23 >> 16);
      WAIT_LGKM0();
      if (lane == 0) flags[F_Q] = 1u;
    }
    STAMP(3);
    unsigned ooff[2];
    wo_offsets(c, 8, g8, c8, ooff);
    const unsigned poff8 = strip_off(lane, 8);
    const rsrc_t ro = mk_rsrc(p.wo, NQ * D * 2);
    lds_wait_slow(&flags[F_OP], 1u);
    STAMP(4);
    v4u WO[2];
    WO[0] = ld_nt(ro, ooff[0]); WO[1] = ld_nt(ro, ooff[1]);
    PIN();
    {
      const unsigned pb = (unsigned)(WS_OP + (kvh * 16 + (lane & 15)) * 1152);
      unsigned off[2] = {pb + (4 * sp + (lane >> 4)) * 16, pb + (64 + (sp >> 3)) * 16};
      bool live[2] = {true, true}; v4u g[2];
      __builtin_amdgcn_s_sleep(DLY_O);
      poll<2>(rws, off, live, tag, g);
      const float m = asf(g[1].x | (g[1].y >> 10)), l = asf(g[1].z | (g[1].w >> 10));
      const float M = row_max16(m);
      const float wgt = __builtin_amdgcn_exp2f(m - M);
      const float Ls = row_sum16(wgt * l);
      const float o = tr_sum16(wgt * asf4(g[0]), lane) * __builtin_amdgcn_rcpf(Ls);
      if ((lane & 3) == 0) ((LAS unsigned*)(L + L_OS))[lane >> 2] = cvtpk(o, o);
      WAIT_LGKM0();
    }
    STAMP(5);
    wo_partial(c, poff8, *(const LAS v2u*)(L + L_OS + 8 * g8), WO, sel, tag);
    if (lg < 18) {
      lds_wait_slow(&flags[C_SP], 9u);
      __builtin_amdgcn_s_sleep(DLY_S);
      const float sv = final_sum(rws, (unsigned)(WS_SP + rfin * 16), lane, tag);
      if (lane < 16 && (lane & 3) == 0) ((LAS float*)(L + L_FIN))[lane >> 2] = sv;
      WAIT_LGKM0();
      if (lane == 0) flags[F_S] = 1u;
      STAMP(6);
    }
  } else {
    const Tag tag = mk_tag(flags[W_TAG]);
    int late;
    {
      wait_until(t_start + TB_READ);
      const v4u g = untag(ld_sc1(rws, lane < 8 ? (unsigned)(WS_TB + lane * 128) : OOB), tag);
      const bool ok = lane < 8 && (resid(g) & TMASK) == 0u;
      late = ok ? (int)(t_start - (g.x | (g.y >> 10))) : 0;
      late = late > TB_MAX ? 0 : late;
      late = max(late, __builtin_amdgcn_mov_dpp(late, XOR1, 0xf, 0xf, true));
      late = max(late, __builtin_amdgcn_mov_dpp(late, XOR2, 0xf, 0xf, true));
      late = max(late, __builtin_amdgcn_mov_dpp(late, XOR7, 0xf, 0xf, true));
      late = UNI(late);
      if (lane == 0 && late > 0) flags[W_SHIFT] = (unsigned)late;
    }
    asm volatile("" :: "v"(cpf0), "v"(cpf1), "v"(cpf2), "v"(tl0), "v"(tl1), "v"(tl2), "v"(tl3));
    const Sel sel = mk_sel(lane);
    const int g0 = 9 * (idx >> 1) + 4 * (idx & 1), odd = idx & 1;
    unsigned doff[3];
    wd_offsets(c, 8, g8, c8, doff);
    const unsigned poff8 = strip_off(lane, 8);
    const rsrc_t rd = mk_rsrc(p.wd, F * D * 2);
    const bool lc = odd && lane < 32;
    const unsigned pb = (unsigned)(WS_GP + (xg * 32 + (lane & 15)) * 2304);
    unsigned off[3] = {pb + (g0 + (lane >> 4)) * 16, pb + 16 * 2304 + (g0 + (lane >> 4)) * 16, lc ? pb + (lane >> 4) * (16 * 2304) + (g0 + 4) * 16 : OOB};
    bool live[3] = {true, true, lc}; v4u g[3];
    const int vi = (lane >> 2) & 3;
    const int col = 64 * (rfin >> 4) + 32 * ((rfin >> 3) & 1) + 8 * vi + (rfin & 7);
    wait_until(t_start + T_LAST - (unsigned)late);
    v4u WD[3];
#pragma unroll
    for (int t = 0; t < 3; ++t) WD[t] = ld_nt(rd, doff[t]);
    PIN();
    lds_wait(&flags[C_GU], 9u);
    STAMP(1);
    __builtin_amdgcn_s_sleep(DLY_H);
    poll<3>(rws, off, live, tag, g);
    STAMP(2);
    if (!lc) g[2] = v4u{0u, 0u, 0u, 0u};
    {
      const float gs = tr_sum16(asf4(g[0]), lane), us = tr_sum16(asf4(g[1]), lane), cs = tr_sum16(asf4(g[2]), lane);
      auto sw = __builtin_amdgcn_permlane16_swap(fbits(cs), fbits(cs), false, false);
      const bool ex = (lane & 3) == 1;
      const float gin = rbf(ex ? asf(sw[0]) : gs), uin = rbf(ex ? asf(sw[1]) : us);
      const float hv = rbf(silu_f(gin)) * uin;
      LAS unsigned* hs = (LAS unsigned*)(L + L_HS);
      if ((lane & 3) == 0 || (ex && lane < 16)) hs[ex ? 16 + vi : lane >> 2] = cvtpk(hv, hv);
    }
    WAIT_LGKM0();
    STAMP(3);
    wd_partial(c, poff8, *(const LAS v2u*)(L + L_HS + 8 * g8), ((const LAS unsigned*)(L + L_HS))[16 + (g8 & 3)], WD, sel, tag);
    if (lg < 18) {
      const float xr = ((const LAS float*)(L + L_XR))[vi];
      lds_wait(&flags[C_FP], 9u);
      STAMP(4);
      __builtin_amdgcn_s_sleep(DLY_F);
      const float fv = final_sum(rws, (unsigned)(WS_FP + rfin * 16), lane, tag);
      STAMP(5);
      const float gate = rbf(silu_f(rbf(fv)));
      lds_wait(&flags[F_S], 1u);
      const float sv = ((const LAS float*)(L + L_FIN))[vi];
      const float res = xr + rbf(rbf(sv) * gate);
      if (lane < 16 && (lane & 3) == 0) p.out[col] = (u16)cvtpk(res, res);
      if (b == 0 && lane == 0) __hip_atomic_store((GAS unsigned*)(p.ws + WS_EPOCH), flags[W_TAG], __ATOMIC_RELAXED, __HIP_MEMORY_SCOPE_AGENT);
      STAMP(6);
    }
  }
  } else {
  const int st = wave - 1;
  const Sel sel = mk_sel(lane);
  const rsrc_t rg = mk_rsrc((idx >> 4) ? p.wu : p.wg, D * F * 2);
  unsigned goff, goff4, ooff[2], doff[3];
  auto late_addr = [&]() {
    PIN();
    int ln = lane; asm volatile("" : "+v"(ln));
    const int g8_ = ln >> 3, c8_ = ln & 7;
    const unsigned gcol = (unsigned)(576 * xg + 64 * st + 8 * c8_);
    goff = (unsigned)(((36 * sp + 4 * g8_) * F + gcol) * 2);
    goff4 = g8_ < 4 ? (unsigned)(((36 * sp + 32 + g8_) * F + gcol) * 2) : OOB;
    wo_offsets(c, st, g8_, c8_, ooff);
    wd_offsets(c, st, g8_, c8_, doff);
    PIN();
  };
  const unsigned poff = strip_off(lane, st);
  v4u G[5];
  unsigned tn;
#define SLOT(dt) do { wait_until(tn); tn += (dt); } while (0)
  auto issue_g = [&](int t) { SLOT(DT2); G[t] = ld_nt(rg, t < 4 ? goff + t * (F * 2) : goff4); PIN(); };
  v4u gb[5];
  auto gu_frags = [&]() {
    const v4u x4 = *(const LAS v4u*)(L + L_XG + 16 * g8);
    const unsigned x5 = ((const LAS unsigned*)(L + L_XG))[32 + g8];
#pragma unroll
    for (int t = 0; t < 4; ++t) gb[t] = sel_b(x4[t], sel);
    gb[4] = sel_b(x5, sel);
    PIN();
  };
  auto do_gu = [&](const Tag& tag) {
    f32x4 acc = {0.f, 0.f, 0.f, 0.f};
#pragma unroll
    for (int t = 0; t < 5; ++t) acc = mfma(G[t], gb[t], acc);
    publish_at(c, poff + WS_GP + (xg * 32 + idx) * 2304, acc, tag);
    if (lane == 0) lds_add(&flags[C_GU]);
    PIN();
  };

  if (wave > 8) {
    tn = now32() + TSTART + 2 * DT1;
    late_addr();
#pragma unroll
    for (int t = 0; t < 5; ++t) { issue_g(t); tn += W9GAP - DT2; }
    lds_wait_slow(&flags[F_XN], 1u);
    const Tag tag = mk_tag(flags[W_TAG]);
    gu_frags();
    do_gu(tag);
    STAMP(2);
    lds_wait(&flags[C_ATT], 8u);
    STAMP(3);
    const int hh = g8 & 1;
    f32x2 ml[8]; f32x4 po[8];
#pragma unroll
    for (int w = 0; w < 8; ++w) { ml[w] = ((const LAS f32x2*)(L + L_ML))[w * 2 + hh]; po[w] = ((const LAS f32x4*)(L + L_PO))[w * 64 + lane]; }
    const float M = fmaxf(fmaxf(fmaxf(ml[0].x, ml[1].x), fmaxf(ml[2].x, ml[3].x)), fmaxf(fmaxf(ml[4].x, ml[5].x), fmaxf(ml[6].x, ml[7].x)));
    f32x4 o = {0.f, 0.f, 0.f, 0.f}; float Ls = 0.f;
#pragma unroll
    for (int w = 0; w < 8; ++w) {
      const float sc = __builtin_amdgcn_exp2f(ml[w].x - M);
      Ls = fmaf(sc, ml[w].y, Ls);
      o += sc * po[w];
    }
    const unsigned pbase = (unsigned)(WS_OP + (kvh * 16 + sp) * 1152);
    st_sc1(rws, pbase + (hh * 32 + (lane >> 4) * 8 + c8) * 16, pack4(o, tag));
    if ((lane & 0x37) == 0) st_sc1(rws, pbase + (64 + hh) * 16, pack2x(fbits(M), fbits(Ls), tag));
    if (lane == 0) flags[F_OP] = 1u;
    PIN();
    STAMP(4);
  } else {
    const size_t tile = ((size_t)kvh * SK + (SK - WIN)) * DH;
    const rsrc_t rk = mk_rsrc(p.kc + tile, WIN * DH * 2);
    const rsrc_t rv = mk_rsrc(p.vc + tile, WIN * DH * 2);
    const int cbase = (st * 8 * 16 + sp) * 1024;
    const unsigned koff = (unsigned)(((lane >> 2) & 3) * 256 + ((lane & 3) + 4 * (lane >> 4)) * 16);
    STAMP(13);
    tn = now32() + TSTART + (st * DT1) / 8;
    v4u K[8], V[8];
#define LDK(t) do { SLOT(DT1); if (t >= KWIN) WAIT_VM(KWIN - 1); K[t] = __builtin_amdgcn_raw_buffer_load_b128(rk, (int)koff, cbase + t * 16384, AUX_NT); PIN(); } while (0)
#define LDV(t) do { SLOT(DT1); WAIT_VM(KWIN - 1); V[t] = __builtin_amdgcn_raw_buffer_load_b128(rv, lane * 16, cbase + t * 16384, AUX_NT); PIN(); } while (0)
    unsigned shifted = 0;
#define CATCH_UP() do { const unsigned sh_ = UNI(flags[W_SHIFT]); tn -= sh_ - shifted; shifted = sh_; } while (0)
#pragma unroll
    for (int t = 0; t < 8; ++t) { LDK(t); if (t == 1) late_addr(); if (t == 5) CATCH_UP(); }
    constexpr float C2 = 0.08838834764831845f * 1.4426950408889634f;
    unsigned pk[8];
    auto qk_softmax = [&]() {
      const v4u qf = *(const LAS v4u*)(L + L_QB + ((g8 & 1) * 128 + ((lane & 3) + 4 * (lane >> 4)) * 8) * 2);
      float s[8];
#pragma unroll
      for (int t = 0; t < 8; ++t) {
        const f32x4 d = mfma(K[t], qf, f32x4{0.f, 0.f, 0.f, 0.f});
        s[t] = (dpp<0x00>(d[0]) + dpp<0x55>(d[1])) + (dpp<0xAA>(d[2]) + dpp<0xFF>(d[3]));
      }
      float m = fmaxf(fmaxf(fmaxf(s[0], s[1]), fmaxf(s[2], s[3])), fmaxf(fmaxf(s[4], s[5]), fmaxf(s[6], s[7])));
      m = x16_max(m) * C2;
      float l = 0.f;
#pragma unroll
      for (int t = 0; t < 8; ++t) { const float pr = __builtin_amdgcn_exp2f(fmaf(s[t], C2, -m)); l += pr; pk[t] = cvtpk(pr, pr); }
      l = x16_sum(l);
      if ((lane & 0x37) == 0) ((LAS f32x2*)(L + L_ML))[st * 2 + g8] = f32x2{m, l};
      PIN();
    };
    auto pv = [&]() {
      f32x4 oacc = {0.f, 0.f, 0.f, 0.f};
#pragma unroll
      for (int t = 0; t < 8; ++t) oacc = mfma(V[t], sel_b(pk[t], sel), oacc);
      ((LAS f32x4*)(L + L_PO))[st * 64 + lane] = oacc;
      WAIT_LGKM0();
      if (lane == 0) lds_add(&flags[C_ATT]);
      PIN();
    };
#pragma unroll
    for (int t = 0; t < QKPOS; ++t) { LDV(t); if (t == 0 || t == 3) CATCH_UP(); }
    const bool early = UNI(flags[F_Q]) != 0u;
    if (early) qk_softmax();
    STAMP(3);
#pragma unroll
    for (int t = QKPOS; t < 8; ++t) LDV(t);
    STAMP(11);
    tn -= (st * DT1) / 8;
    issue_g(0); issue_g(1);
    STAMP(1);
    if (early) pv();
    STAMP(4);
    issue_g(2); issue_g(3); issue_g(4);
    if (!early) lds_wait(&flags[F_XN], 1u);
    const Tag tag = mk_tag(flags[W_TAG]);
    gu_frags();
    const rsrc_t ro = mk_rsrc(p.wo, NQ * D * 2);
    const rsrc_t rd = mk_rsrc(p.wd, F * D * 2);
    v4u WO[2], WD[3];
    f32x4 gacc = {0.f, 0.f, 0.f, 0.f};
#pragma unroll
    for (int t = 0; t < 3; ++t) gacc = mfma(G[t], gb[t], gacc);
    PIN();
    tn += DTD - DT2;
    SLOT(DTD); WD[0] = ld_nt(rd, doff[0]); PIN();
    STAMP(5);
    gacc = mfma(G[3], gb[3], gacc);
    gacc = mfma(G[4], gb[4], gacc);
    publish_at(c, poff + WS_GP + (xg * 32 + idx) * 2304, gacc, tag);
    if (lane == 0) lds_add(&flags[C_GU]);
    PIN();
    STAMP(6);
    if (!early) { lds_wait(&flags[F_Q], 1u); qk_softmax(); pv(); }
    STAMP(2);
    SLOT(DTD); WD[1] = ld_nt(rd, doff[1]); PIN();
    if (idx & 1) SLOT(DTD);
    WD[2] = ld_nt(rd, doff[2]); PIN();
    tn += DTO - DTD;
    SLOT(DT2); WO[0] = ld_nt(ro, ooff[0]); PIN();
    SLOT(DT2); WO[1] = ld_nt(ro, ooff[1]); PIN();
    v2u h2, o2; unsigned h3, spins = 0; bool hh;
#pragma nounroll
    for (;;) {
      h2 = *(const volatile LAS v2u*)(L + L_HS + 8 * g8); h3 = ((const volatile LAS unsigned*)(L + L_HS))[16 + (g8 & 3)];
      o2 = *(const volatile LAS v2u*)(L + L_OS + 8 * g8);
      hh = __all(h2.x != EMPTY && h2.y != EMPTY && h3 != EMPTY);
      if (hh || __all(o2.x != EMPTY && o2.y != EMPTY) || ++spins > (SPIN_CAP << 6)) break;
    }
    if (hh) {
      wd_partial(c, poff, h2, h3, WD, sel, tag);
      STAMP(8);
#pragma nounroll
      while (!__all(o2.x != EMPTY && o2.y != EMPTY) && ++spins <= (SPIN_CAP << 6)) o2 = *(const volatile LAS v2u*)(L + L_OS + 8 * g8);
      wo_partial(c, poff, o2, WO, sel, tag);
      STAMP(7);
    } else {
      wo_partial(c, poff, o2, WO, sel, tag);
      STAMP(7);
#pragma nounroll
      while (!__all(h2.x != EMPTY && h2.y != EMPTY && h3 != EMPTY) && ++spins <= (SPIN_CAP << 6)) {
        h2 = *(const volatile LAS v2u*)(L + L_HS + 8 * g8); h3 = ((const volatile LAS unsigned*)(L + L_HS))[16 + (g8 & 3)];
      }
      wd_partial(c, poff, h2, h3, WD, sel, tag);
      STAMP(8);
    }
  }
  }
  STAMP_FLUSH();
}

}

extern "C" void kernel_launch(void* const* d_in, const int* in_sizes, int n_in, void* d_out, int out_size, void* d_ws, size_t ws_size, hipStream_t stream) {
  Params p;
  p.x = (const u16*)d_in[0]; p.kc = (const u16*)d_in[1]; p.vc = (const u16*)d_in[2];
  p.wq = (const u16*)d_in[3]; p.wo = (const u16*)d_in[4];
  p.wg = (const u16*)d_in[6]; p.wu = (const u16*)d_in[7]; p.wd = (const u16*)d_in[8];
  p.out = (u16*)d_out; p.ws = (unsigned char*)d_ws;
  (void)hipFuncSetAttribute((const void*)fused_decode, hipFuncAttributeMaxDynamicSharedMemorySize, LDS_BYTES);
  hipLaunchKernelGGL(fused_decode, dim3(NWG), dim3(NTHR), LDS_BYTES, stream, p);
}
```

```cpp
#include <hip/hip_runtime.h>
#include <hip/hip_bf16.h>
#include <stdint.h>

#define GAS __attribute__((address_space(1)))
#define LAS __attribute__((address_space(3)))
typedef unsigned v4u __attribute__((ext_vector_type(4)));
typedef unsigned v2u __attribute__((ext_vector_type(2)));
typedef float f32x4 __attribute__((ext_vector_type(4)));
typedef float f32x2 __attribute__((ext_vector_type(2)));
typedef short bf16x8 __attribute__((ext_vector_type(8)));
typedef __bf16 bf16x2 __attribute__((ext_vector_type(2)));
typedef unsigned short u16;
typedef __amdgpu_buffer_rsrc_t rsrc_t;

#define KWIN 4
#define DT1 34
#define DT2 36
#define DTD 36
#define DTO 36
#define DRY0 0
#define OUTP 1
#define TLBP 0
#define TSTART 10
#define W9GAP 70
#define T_Q 450
#define T_LAST 790
#define CODE_BYTES 18944
#define CODE_PF_WGS 2
#define TB_READ 170
#define TB_MAX 300
#define DLY_Q 24
#define DLY_O 30
#define DLY_H 20
#define DLY_F 20
#define DLY_S 28
#define RETRY 8

namespace {

constexpr int D = 576, SK = 65536, DH = 128, WIN = 4096, F = 4608, NQ = 4096;
constexpr int NWG = 256, NWAVE = 11, NTHR = NWAVE * 64;
constexpr int LDS_BYTES = 84 * 1024;

constexpr int WS_EPOCH = 0;
constexpr int WS_TB = 256;
constexpr int WS_QP = WS_TB + 1024;
constexpr int WS_OP = WS_QP + 16 * 4096;
constexpr int WS_GP = WS_OP + 16 * 16 * 1152;
constexpr int WS_SP = WS_GP + 8 * 32 * 2304;
constexpr int WS_FP = WS_SP + 256 * 2304;
constexpr int WS_END = WS_FP + 256 * 2304;

constexpr int L_FLAG = 0;
constexpr int L_OS = 64;
constexpr int L_HS = L_OS + 64;
constexpr int L_XQ = L_HS + 128;
constexpr int L_XG = L_XQ + 576;
constexpr int L_XR = L_XG + 160;
constexpr int L_QB = L_XR + 16;
constexpr int L_ML = L_QB + 512;
constexpr int L_FIN = L_ML + 128;
constexpr int L_PO = L_FIN + 16;
constexpr int L_X = L_PO + 8192;
constexpr int L_WQ = L_X + 2048;
constexpr int L_TS = L_WQ + 18 * 1024;
static_assert(L_PO % 16 == 0 && L_XQ % 16 == 0 && L_XG % 16 == 0 && L_QB % 16 == 0 && L_OS % 16 == 0 && L_HS % 16 == 0 && L_XR % 16 == 0 && L_X % 16 == 0, "align");
enum { F_XN = 0, F_Q = 1, C_ATT = 2, C_GU = 3, F_OP = 4, C_SP = 5, C_FP = 6, F_S = 7, W_TAG = 8, W_SHIFT = 9, F_X = 10, F_WQ = 11 };

constexpr int AUX_NT = 2, AUX_SC1 = 16;
constexpr unsigned VMASK = 0xFFFFFC00u, TMASK = 0x3FFu, OOB = 0xFFFFFF00u;
constexpr unsigned EMPTY = 0xFFFF0000u;
constexpr unsigned SPIN_CAP = 1u << 17;

struct Params {
  const u16* x; const u16* kc; const u16* vc; const u16* wq; const u16* wo;
  const u16* wg; const u16* wu; const u16* wd; u16* out; unsigned char* ws;
};

__device__ __forceinline__ unsigned fbits(float f) { return __builtin_bit_cast(unsigned, f); }
__device__ __forceinline__ float asf(unsigned u) { return __builtin_bit_cast(float, u); }
__device__ __forceinline__ unsigned cvtpk(float a, float b) { bf16x2 r = {(__bf16)a, (__bf16)b}; return __builtin_bit_cast(unsigned, r); }
__device__ __forceinline__ float rbf(float f) { return asf(cvtpk(0.f, f) & 0xffff0000u); }
__device__ __forceinline__ float silu_f(float x) { return x * __builtin_amdgcn_rcpf(1.f + __builtin_amdgcn_exp2f(x * -1.4426950408889634f)); }
__device__ __forceinline__ f32x4 mfma(v4u a, v4u b, f32x4 c) {
  return __builtin_amdgcn_mfma_f32_16x16x32_bf16(__builtin_bit_cast(bf16x8, a), __builtin_bit_cast(bf16x8, b), c, 0, 0, 0);
}
template <int CTRL> __device__ __forceinline__ float dpp(float x) {
  return asf(__builtin_amdgcn_mov_dpp(__builtin_bit_cast(int, x), CTRL, 0xf, 0xf, true));
}
template <int CTRL, int RM> __device__ __forceinline__ float dppm(float x) {
  return asf(__builtin_amdgcn_update_dpp(0, __builtin_bit_cast(int, x), CTRL, RM, 0xf, true));
}
constexpr int XOR1 = 0xB1, XOR2 = 0x4E, XOR7 = 0x141, ROR8 = 0x128;
__device__ __forceinline__ float x16_max(float x) {
  auto s = __builtin_amdgcn_permlane16_swap(fbits(x), fbits(x), false, false);
  x = fmaxf(asf(s[0]), asf(s[1]));
  auto t = __builtin_amdgcn_permlane32_swap(fbits(x), fbits(x), false, false);
  return fmaxf(asf(t[0]), asf(t[1]));
}
__device__ __forceinline__ float x16_sum(float x) {
  auto s = __builtin_amdgcn_permlane16_swap(fbits(x), fbits(x), false, false);
  x = asf(s[0]) + asf(s[1]);
  auto t = __builtin_amdgcn_permlane32_swap(fbits(x), fbits(x), false, false);
  return asf(t[0]) + asf(t[1]);
}
__device__ __forceinline__ float row_sum16(float x) { x += dpp<XOR1>(x); x += dpp<XOR2>(x); x += dpp<XOR7>(x); x += dpp<ROR8>(x); return x; }
__device__ __forceinline__ float row_max16(float x) { x = fmaxf(x, dpp<XOR1>(x)); x = fmaxf(x, dpp<XOR2>(x)); x = fmaxf(x, dpp<XOR7>(x)); x = fmaxf(x, dpp<ROR8>(x)); return x; }
__device__ __forceinline__ float wave_sum_s(float s) {
  s += dpp<0x111>(s); s += dpp<0x112>(s); s += dpp<0x114>(s); s += dpp<0x118>(s);
  s += dppm<0x142, 0xa>(s); s += dppm<0x143, 0xc>(s);
  return asf(__builtin_amdgcn_readlane(fbits(s), 63));
}
__device__ __forceinline__ float tr_sum16(f32x4 v, int lane) {
  const bool b3 = lane & 8, b2 = lane & 4;
  const float a = (b3 ? v[2] : v[0]) + dpp<ROR8>(b3 ? v[0] : v[2]);
  const float b = (b3 ? v[3] : v[1]) + dpp<ROR8>(b3 ? v[1] : v[3]);
  float r = (b2 ? b : a) + dpp<XOR7>(b2 ? a : b);
  r += dpp<XOR2>(r); r += dpp<XOR1>(r);
  return r;
}

__device__ __forceinline__ rsrc_t mk_rsrc(const void* p, unsigned bytes) {
  return __builtin_amdgcn_make_buffer_rsrc((void*)p, 0, (int)bytes, 0x00020000);
}
__device__ __forceinline__ v4u ld_nt(rsrc_t r, unsigned off) { return __builtin_amdgcn_raw_buffer_load_b128(r, (int)off, 0, AUX_NT); }
__device__ __forceinline__ v4u ld_sc1(rsrc_t r, unsigned off) { return __builtin_amdgcn_raw_buffer_load_b128(r, (int)off, 0, AUX_SC1); }
__device__ __forceinline__ void st_sc1(rsrc_t r, unsigned off, v4u g) { __builtin_amdgcn_raw_buffer_store_b128(g, r, (int)off, 0, AUX_SC1); }
__device__ __forceinline__ unsigned now32() { unsigned long long t; asm volatile("s_memrealtime %0\n\ts_waitcnt lgkmcnt(0)" : "=s"(t) :: "memory"); return (unsigned)t; }
__device__ __forceinline__ void wait_until(unsigned t) {
#pragma nounroll
  while ((int)(now32() - t) < 0) __builtin_amdgcn_s_sleep(1);
}
#define PIN() __builtin_amdgcn_sched_barrier(0)
#define CBAR() asm volatile("" ::: "memory")
#define WAIT_VM(n) asm volatile("s_waitcnt vmcnt(%0)" ::"i"(n) : "memory")
#define WAIT_LGKM0() asm volatile("s_waitcnt lgkmcnt(0)" ::: "memory")
#define UNI(x) __builtin_amdgcn_readfirstlane(x)

struct Tag { unsigned t0, t1, t2, t3; };
__device__ __forceinline__ Tag mk_tag(unsigned T) { return Tag{T & TMASK, (T + 0x155u) & TMASK, (T + 0x2AAu) & TMASK, (T + 0x0FFu) & TMASK}; }
__device__ __forceinline__ v4u pack4(f32x4 v, const Tag& t) {
  return v4u{(fbits(v.x) & VMASK) | t.t0, (fbits(v.y) & VMASK) | t.t1, (fbits(v.z) & VMASK) | t.t2, (fbits(v.w) & VMASK) | t.t3};
}
__device__ __forceinline__ v4u pack2x(unsigned a, unsigned b, const Tag& t) {
  return v4u{(a & VMASK) | t.t0, ((a & TMASK) << 10) | t.t1, (b & VMASK) | t.t2, ((b & TMASK) << 10) | t.t3};
}
__device__ __forceinline__ v4u untag(v4u g, const Tag& t) { return v4u{g.x ^ t.t0, g.y ^ t.t1, g.z ^ t.t2, g.w ^ t.t3}; }
__device__ __forceinline__ unsigned resid(v4u u) { return (u.x | u.y | u.z) | u.w; }
__device__ __forceinline__ f32x4 asf4(v4u u) { return __builtin_bit_cast(f32x4, u); }

template <int N>
__device__ __forceinline__ void poll(rsrc_t r, const unsigned (&off)[N], const bool (&live)[N], const Tag& tag, v4u (&res)[N]) {
  unsigned spins = 0;
#pragma nounroll
  for (;;) {
    v4u a[N];
#pragma unroll
    for (int i = 0; i < N; ++i) a[i] = ld_sc1(r, off[i]);
    CBAR();
    unsigned bad = 0;
#pragma unroll
    for (int i = 0; i < N; ++i) { res[i] = untag(a[i], tag); bad |= live[i] ? resid(res[i]) : 0u; }
    if (__all((bad & TMASK) == 0u) || ++spins > SPIN_CAP) break;
    __builtin_amdgcn_s_sleep(RETRY);
  }
}
__device__ __forceinline__ float final_sum(rsrc_t rws, unsigned base, int lane, const Tag& tag) {
  unsigned off[4]; bool live[4]; v4u g[4];
#pragma unroll
  for (int i = 0; i < 4; ++i) { off[i] = base + (unsigned)((i * 64 + lane) * 2304); live[i] = true; }
  poll<4>(rws, off, live, tag, g);
  const f32x4 v = (asf4(g[0]) + asf4(g[1])) + (asf4(g[2]) + asf4(g[3]));
  return x16_sum(tr_sum16(v, lane));
}

__device__ __forceinline__ void lds_wait(volatile LAS unsigned* f, unsigned want) {
  unsigned spins = 0;
#pragma nounroll
  while (*f < want) { if (++spins > (SPIN_CAP << 6)) break; }
  CBAR();
}
__device__ __forceinline__ void lds_wait_slow(volatile LAS unsigned* f, unsigned want) {
  unsigned spins = 0;
#pragma nounroll
  while (*f < want) { __builtin_amdgcn_s_sleep(2); if (++spins > (SPIN_CAP << 4)) break; }
  CBAR();
}
__device__ __forceinline__ void lds_add(volatile LAS unsigned* f) { __hip_atomic_fetch_add((LAS unsigned*)f, 1u, __ATOMIC_RELAXED, __HIP_MEMORY_SCOPE_WORKGROUP); }

struct Sel { unsigned m0, m1, m2, m3; };
__device__ __forceinline__ Sel mk_sel(int lane) {
  const unsigned h = (lane & 1) ? 0xFFFF0000u : 0x0000FFFFu; const int r = (lane & 7) >> 1;
  return Sel{r == 0 ? h : 0u, r == 1 ? h : 0u, r == 2 ? h : 0u, r == 3 ? h : 0u};
}
__device__ __forceinline__ v4u sel_b(unsigned dup, const Sel& s) { return v4u{dup & s.m0, dup & s.m1, dup & s.m2, dup & s.m3}; }
__device__ __forceinline__ f32x4 strip_fold(f32x4 d) {
  f32x4 y;
#pragma unroll
  for (int i = 0; i < 4; ++i) {
    const float t = dpp<ROR8>(d[i]);
    auto s = __builtin_amdgcn_permlane32_swap(fbits(t), fbits(t), false, false);
    y[i] = d[i] + asf(s[1]);
  }
  return y;
}

struct Ctx {
  LAS unsigned char* L; volatile LAS unsigned* flags; rsrc_t rws;
  int lane, g8, c8, b, xg, idx, kvh, sp;
};
__device__ __forceinline__ void wo_offsets(const Ctx& c, int st, int g8, int c8, unsigned (&off)[2]) {
  const int hrow = (2 * c.kvh + (c.sp >> 3)) * 128 + 32 * ((c.sp >> 1) & 3) + 4 * (c.sp & 1);
#pragma unroll
  for (int t = 0; t < 2; ++t) { const int u = 2 * g8 + t; off[t] = (unsigned)(((hrow + 8 * (u & 3) + (u >> 2)) * D + 64 * st + 8 * c8) * 2); }
}
__device__ __forceinline__ void wd_offsets(const Ctx& c, int st, int g8, int c8, unsigned (&off)[3]) {
  const int odd = c.idx & 1, g0 = 9 * (c.idx >> 1) + 4 * odd;
#pragma unroll
  for (int t = 0; t < 3; ++t) {
    const int u = t < 2 ? 2 * g8 + t : 16 + (g8 & 3), Gi = g0 + (u >> 2);
    const int row = 576 * c.xg + 64 * (Gi >> 4) + 32 * ((Gi >> 3) & 1) + 8 * (u & 3) + (Gi & 7);
    off[t] = (t < 2 || (odd && g8 < 4)) ? (unsigned)((row * D + 64 * st + 8 * c8) * 2) : OOB;
  }
}
__device__ __forceinline__ unsigned strip_off(int lane, int st) {
  unsigned o = (lane < 32 && (lane & 15) < 8) ? (unsigned)((st * 16 + (lane >> 4) * 8 + (lane & 7)) * 16) : 0x80000000u;
  asm volatile("" : "+v"(o));
  return o;
}
__device__ __forceinline__ void publish_at(const Ctx& c, unsigned off, f32x4 acc, const Tag& tag) {
  st_sc1(c.rws, off, pack4(strip_fold(acc), tag));
}
__device__ __forceinline__ void wo_partial(const Ctx& c, unsigned poff, v2u o2, const v4u (&WO)[2], const Sel& sel, const Tag& tag) {
  f32x4 acc = {0.f, 0.f, 0.f, 0.f};
  acc = mfma(WO[0], sel_b(o2.x, sel), acc);
  acc = mfma(WO[1], sel_b(o2.y, sel), acc);
  publish_at(c, poff + WS_SP + c.b * 2304, acc, tag);
  if (c.lane == 0) lds_add(&c.flags[C_SP]);
  PIN();
}
__device__ __forceinline__ void wd_partial(const Ctx& c, unsigned poff, v2u h2, unsigned h3, const v4u (&WD)[3], const Sel& sel, const Tag& tag) {
  f32x4 acc = {0.f, 0.f, 0.f, 0.f};
  acc = mfma(WD[0], sel_b(h2.x, sel), acc);
  acc = mfma(WD[1], sel_b(h2.y, sel), acc);
  acc = mfma(WD[2], sel_b(c.g8 < 4 ? h3 : 0u, sel), acc);
  publish_at(c, poff + WS_FP + c.b * 2304, acc, tag);
  if (c.lane == 0) lds_add(&c.flags[C_FP]);
  PIN();
}

#define STAMP(i) do {} while (0)
#define STAMP_INIT() do {} while (0)
#define STAMP_FLUSH() do {} while (0)

__global__ void __launch_bounds__(NTHR, 1) fused_decode(Params p) {
  extern __shared__ __attribute__((aligned(16))) unsigned char smem[];
  LAS unsigned char* L = (LAS unsigned char*)smem;
  volatile LAS unsigned* flags = (volatile LAS unsigned*)(L + L_FLAG);
  const int lane = threadIdx.x & 63;
  const int wave = UNI(threadIdx.x >> 6);
  const int b = blockIdx.x, xg = b & 7, idx = b >> 3;
  const int kvh = 2 * xg + (idx >> 4), sp = idx & 15;
  const rsrc_t rws = mk_rsrc(p.ws, WS_END);
  const int lg = (idx >> 3) * 8 + xg;
  const int rfin = lg * 8 + (idx & 7);
  const int g8 = lane >> 3, c8 = lane & 7;
  const Ctx c{L, flags, rws, lane, g8, c8, b, xg, idx, kvh, sp};

  unsigned long long code_base;
  asm volatile("s_getpc_b64 %0" : "=s"(code_base));
  code_base &= ~255ull;
  unsigned cpf0 = 0, cpf1 = 0, cpf2 = 0;
  if ((threadIdx.x >> 6) == 10 && (int)(blockIdx.x >> 3) < CODE_PF_WGS) {
    const GAS unsigned* cp = (const GAS unsigned*)code_base + (threadIdx.x & 63) * 32;
    if ((threadIdx.x & 63) * 128 + 16384 < CODE_BYTES) cpf2 = cp[4096];
    if ((threadIdx.x & 63) * 128 + 8192 < CODE_BYTES) cpf1 = cp[2048];
    cpf0 = cp[0];
  }
  PIN();
  STAMP_INIT();
  STAMP(0);
  const unsigned t_start = now32();
  asm volatile("" :: "s"(p.x), "s"(p.kc), "s"(p.vc), "s"(p.wq), "s"(p.wo), "s"(p.wg), "s"(p.wu), "s"(p.wd), "s"(p.out), "s"(p.ws));
  if (wave == 0 && lane < 52) flags[lane] = lane < 16 ? 0u : EMPTY;
  WAIT_LGKM0();
  __builtin_amdgcn_s_barrier();
  PIN();
  STAMP(12);

  unsigned tl0 = 0, tl1 = 0, tl2 = 0, tl3 = 0;
  if (wave == 10) {
    const unsigned E = __hip_atomic_load((const GAS unsigned*)(p.ws + WS_EPOCH), __ATOMIC_RELAXED, __HIP_MEMORY_SCOPE_AGENT);
    const rsrc_t rx = mk_rsrc(p.x, D * 2);
    __builtin_amdgcn_raw_ptr_buffer_load_lds(rx, (LAS void*)(L + L_X), 16, lane * 16, 0, 0, 0);
    __builtin_amdgcn_raw_ptr_buffer_load_lds(rx, (LAS void*)(L + L_X + 1024), 16, 1024 + lane * 16, 0, 0, 0);
    const rsrc_t rq = mk_rsrc(p.wq, D * NQ * 2);
    const unsigned qoff = (unsigned)(((144 * (sp >> 2) + 18 * g8) * NQ + kvh * 256 + 64 * (sp & 3) + 8 * c8) * 2);
#pragma unroll
    for (int t = 0; t < 18; ++t) __builtin_amdgcn_raw_ptr_buffer_load_lds(rq, (LAS void*)(L + L_WQ + 1024 * t), 16, qoff + t * (NQ * 2), 0, 0, AUX_NT);
    PIN();
    {
      const rsrc_t rg = mk_rsrc((idx >> 4) ? p.wu : p.wg, D * F * 2);
      tl0 = __builtin_amdgcn_raw_buffer_load_b32(rg, (TLBP & 1) && lane < 36 ? ((36 * sp + lane) * F + 576 * xg) * 2 : OOB, 0, AUX_NT);
      const int odd = idx & 1, Gi = 9 * (idx >> 1) + 4 * odd + (lane >> 2);
      const int drow = 576 * xg + 64 * (Gi >> 4) + 32 * ((Gi >> 3) & 1) + 8 * (lane & 3) + (Gi & 7);
      tl1 = __builtin_amdgcn_raw_buffer_load_b32(mk_rsrc(p.wd, F * D * 2), (TLBP & 2) && lane < 16 + 4 * odd ? drow * D * 2 : OOB, 0, AUX_NT);
      const int orow = (2 * kvh + (sp >> 3)) * 128 + 32 * ((sp >> 1) & 3) + 4 * (sp & 1) + 8 * (lane & 3) + ((lane >> 2) & 3);
      tl2 = __builtin_amdgcn_raw_buffer_load_b32(mk_rsrc(p.wo, NQ * D * 2), (TLBP & 4) && lane < 16 ? orow * D * 2 : OOB, 0, AUX_NT);
      tl3 = __builtin_amdgcn_raw_buffer_load_b32(mk_rsrc(p.vc + ((size_t)kvh * SK + (SK - WIN)) * DH, WIN * DH * 2), (TLBP & 8) ? (lane * 16 + sp) * 1024 : OOB, 0, AUX_NT);
    }
    __builtin_amdgcn_raw_buffer_store_b16((unsigned short)0, mk_rsrc(p.out, D * 2),
                                          (OUTP && lg < 18 && lane < 4) ? (64 * (rfin >> 4) + 32 * ((rfin >> 3) & 1) + 8 * lane + (rfin & 7)) * 2 : (int)OOB, 0, 0);
    PIN();
    STAMP(13);
    __builtin_amdgcn_s_waitcnt(0x4F77);
    const unsigned T = UNI(E) + 1u;
    asm volatile("ds_write_b32 %0, %1 offset:%2\n\ts_waitcnt lgkmcnt(0)\n\tds_write_b32 %0, %3 offset:%4" :: "v"(0u), "v"(T), "i"(L_FLAG + 4 * W_TAG), "v"(1u), "i"(L_FLAG + 4 * F_X) : "memory");
    st_sc1(rws, (idx == 0 && lane == 0) ? (unsigned)(WS_TB + xg * 128) : OOB, pack2x(t_start, 0u, mk_tag(T)));
    __builtin_amdgcn_s_waitcnt(0x0F76);
    asm volatile("ds_write_b32 %0, %1 offset:%2" :: "v"(0u), "v"(1u), "i"(L_FLAG + 4 * F_WQ) : "memory");
    STAMP(14);
  }
  unsigned wave2 = wave;
  asm volatile("" : "+s"(wave2));

  if (wave2 - 1u >= 9u) {
  if (wave2 == 0) {
    const int st = sp & 3, rs = sp >> 2;
    const Sel sel = mk_sel(lane);
    const unsigned qpoff = (lane < 32 && (lane & 15) < 8) ? (unsigned)(WS_QP + ((kvh * 4 + rs) * 4 + st) * 256 + ((lane >> 4) * 8 + c8) * 16) : OOB;
    Tag tag;
    int npass = DRY0 ? 0 : 1; asm volatile("" : "+s"(npass));
#pragma nounroll
    for (int pass = npass; pass < 2; ++pass) {
    if (pass) lds_wait(&flags[F_X], 1u);
    const LAS unsigned* xl = (const LAS unsigned*)(L + L_X);
    const LAS u16* xh = (const LAS u16*)(L + L_X);
    unsigned xw[5];
#pragma unroll
    for (int i = 0; i < 4; ++i) xw[i] = xl[lane + 64 * i];
    xw[4] = lane < 32 ? xl[256 + lane] : 0u;
    const unsigned xq0 = xh[144 * rs + lane], xq1 = xh[144 * rs + 64 + lane], xq2 = xh[144 * rs + 128 + (lane & 15)];
    const unsigned xg0 = xh[36 * sp + (lane < 36 ? lane : 0)];
    const unsigned xr0 = xh[64 * (rfin >> 4) + 32 * ((rfin >> 3) & 1) + 8 * (lane & 3) + (rfin & 7)];
    tag = mk_tag(flags[W_TAG]);
    float xv[10];
#pragma unroll
    for (int i = 0; i < 5; ++i) { xv[2 * i] = asf(xw[i] << 16); xv[2 * i + 1] = asf(xw[i] & 0xffff0000u); }
    const float s = ((xv[0] + xv[1]) + (xv[2] + xv[3])) + ((xv[4] + xv[5]) + (xv[6] + xv[7])) + (xv[8] + xv[9]);
    const float mean = wave_sum_s(s) * (1.f / D);
    float q2 = 0.f, q3 = 0.f;
#pragma unroll
    for (int i = 0; i < 8; i += 2) { const float d0 = xv[i] - mean, d1 = xv[i + 1] - mean; q2 = fmaf(d0, d0, q2); q3 = fmaf(d1, d1, q3); }
    { const float d0 = lane < 32 ? xv[8] - mean : 0.f, d1 = lane < 32 ? xv[9] - mean : 0.f; q2 = fmaf(d0, d0, q2); q3 = fmaf(d1, d1, q3); }
    const float var = wave_sum_s(q2 + q3) * (1.f / D);
    const float mb = rbf(mean);
    const float den = rbf(__builtin_amdgcn_sqrtf(rbf(rbf(var) + 1e-5f)));
    const float rden = __builtin_amdgcn_rcpf(den);
    auto norm = [&](unsigned raw) -> unsigned {
      const float a = rbf(asf(raw << 16) - mb);
      float qv = a * rden; qv = fmaf(fmaf(-qv, den, a), rden, qv);
      return cvtpk(qv, qv);
    };
    LAS unsigned* xq = (LAS unsigned*)(L + L_XQ);
    xq[lane] = norm(xq0); xq[64 + lane] = norm(xq1);
    { const unsigned v2 = norm(xq2); if (lane < 16) xq[128 + lane] = v2; }
    WAIT_LGKM0();
    unsigned xv18[18];
    {
      const LAS unsigned* src = xq + 18 * g8;
#pragma unroll
      for (int i = 0; i < 9; ++i) { const v2u t2 = *(const LAS v2u*)(src + 2 * i); xv18[2 * i] = t2.x; xv18[2 * i + 1] = t2.y; }
    }
    if (pass) STAMP(1);
    f32x4 acc = {0.f, 0.f, 0.f, 0.f};
    if (pass) lds_wait(&flags[F_WQ], 1u);
#pragma unroll
    for (int t = 0; t < 18; ++t) acc = mfma(((const LAS v4u*)(L + L_WQ))[t * 64 + lane], sel_b(xv18[t], sel), acc);
    {
      const f32x4 y = strip_fold(acc);
      st_sc1(rws, pass ? qpoff : OOB, pack4(y, tag));
    }
    if (pass) STAMP(2);
    { const unsigned vg = norm(xg0); if (lane < 40) ((LAS unsigned*)(L + L_XG))[lane] = lane < 36 ? vg : 0u; }
    if (lane < 4) ((LAS float*)(L + L_XR))[lane] = asf(xr0 << 16);
    WAIT_LGKM0();
    if (lane == 0 && pass) flags[F_XN] = 1u;
    }
    {
      unsigned off[4]; bool live[4]; v4u g[4];
#pragma unroll
      for (int r = 0; r < 4; ++r) { off[r] = (unsigned)(WS_QP + (kvh * 4 + r) * 1024 + lane * 16); live[r] = true; }
      __builtin_amdgcn_s_sleep(DLY_Q);
      wait_until(t_start + T_Q - UNI(flags[W_SHIFT]));
      poll<4>(rws, off, live, tag, g);
      const f32x4 q4 = (asf4(g[0]) + asf4(g[1])) + (asf4(g[2]) + asf4(g[3]));
      LAS u16* qb = (LAS u16*)(L + L_QB) + 64 * (lane >> 4) + 32 * (g8 & 1) + c8;
      const unsigned p01 = cvtpk(q4[0], q4[1]), p23 = cvtpk(q4[2], q4[3]);
      qb[0] = (u16)p01; qb[8] = (u16)(p01 >> 16); qb[16] = (u16)p23; qb[24] = (u16)(p23 >> 16);
      WAIT_LGKM0();
      if (lane == 0) flags[F_Q] = 1u;
    }
    STAMP(3);
    unsigned ooff[2];
    wo_offsets(c, 8, g8, c8, ooff);
    const unsigned poff8 = strip_off(lane, 8);
    const rsrc_t ro = mk_rsrc(p.wo, NQ * D * 2);
    lds_wait_slow(&flags[F_OP], 1u);
    STAMP(4);
    v4u WO[2];
    WO[0] = ld_nt(ro, ooff[0]); WO[1] = ld_nt(ro, ooff[1]);
    PIN();
    {
      const unsigned pb = (unsigned)(WS_OP + (kvh * 16 + (lane & 15)) * 1152);
      unsigned off[2] = {pb + (4 * sp + (lane >> 4)) * 16, pb + (64 + (sp >> 3)) * 16};
      bool live[2] = {true, true}; v4u g[2];
      __builtin_amdgcn_s_sleep(DLY_O);
      poll<2>(rws, off, live, tag, g);
      const float m = asf(g[1].x | (g[1].y >> 10)), l = asf(g[1].z | (g[1].w >> 10));
      const float M = row_max16(m);
      const float wgt = __builtin_amdgcn_exp2f(m - M);
      const float Ls = row_sum16(wgt * l);
      const float o = tr_sum16(wgt * asf4(g[0]), lane) * __builtin_amdgcn_rcpf(Ls);
      if ((lane & 3) == 0) ((LAS unsigned*)(L + L_OS))[lane >> 2] = cvtpk(o, o);
      WAIT_LGKM0();
    }
    STAMP(5);
    wo_partial(c, poff8, *(const LAS v2u*)(L + L_OS + 8 * g8), WO, sel, tag);
    if (lg < 18) {
      lds_wait_slow(&flags[C_SP], 9u);
      __builtin_amdgcn_s_sleep(DLY_S);
      const float sv = final_sum(rws, (unsigned)(WS_SP + rfin * 16), lane, tag);
      if (lane < 16 && (lane & 3) == 0) ((LAS float*)(L + L_FIN))[lane >> 2] = sv;
      WAIT_LGKM0();
      if (lane == 0) flags[F_S] = 1u;
      STAMP(6);
    }
  } else {
    const Tag tag = mk_tag(flags[W_TAG]);
    int late;
    {
      wait_until(t_start + TB_READ);
      const v4u g = untag(ld_sc1(rws, lane < 8 ? (unsigned)(WS_TB + lane * 128) : OOB), tag);
      const bool ok = lane < 8 && (resid(g) & TMASK) == 0u;
      late = ok ? (int)(t_start - (g.x | (g.y >> 10))) : 0;
      late = late > TB_MAX ? 0 : late;
      late = max(late, __builtin_amdgcn_mov_dpp(late, XOR1, 0xf, 0xf, true));
      late = max(late, __builtin_amdgcn_mov_dpp(late, XOR2, 0xf, 0xf, true));
      late = max(late, __builtin_amdgcn_mov_dpp(late, XOR7, 0xf, 0xf, true));
      late = UNI(late);
      if (lane == 0 && late > 0) flags[W_SHIFT] = (unsigned)late;
    }
    asm volatile("" :: "v"(cpf0), "v"(cpf1), "v"(cpf2), "v"(tl0), "v"(tl1), "v"(tl2), "v"(tl3));
    const Sel sel = mk_sel(lane);
    const int g0 = 9 * (idx >> 1) + 4 * (idx & 1), odd = idx & 1;
    unsigned doff[3];
    wd_offsets(c, 8, g8, c8, doff);
    const unsigned poff8 = strip_off(lane, 8);
    const rsrc_t rd = mk_rsrc(p.wd, F * D * 2);
    const bool lc = odd && lane < 32;
    const unsigned pb = (unsigned)(WS_GP + (xg * 32 + (lane & 15)) * 2304);
    unsigned off[3] = {pb + (g0 + (lane >> 4)) * 16, pb + 16 * 2304 + (g0 + (lane >> 4)) * 16, lc ? pb + (lane >> 4) * (16 * 2304) + (g0 + 4) * 16 : OOB};
    bool live[3] = {true, true, lc}; v4u g[3];
    const int vi = (lane >> 2) & 3;
    const int col = 64 * (rfin >> 4) + 32 * ((rfin >> 3) & 1) + 8 * vi + (rfin & 7);
    wait_until(t_start + T_LAST - (unsigned)late);
    v4u WD[3];
#pragma unroll
    for (int t = 0; t < 3; ++t) WD[t] = ld_nt(rd, doff[t]);
    PIN();
    lds_wait(&flags[C_GU], 9u);
    STAMP(1);
    __builtin_amdgcn_s_sleep(DLY_H);
    poll<3>(rws, off, live, tag, g);
    STAMP(2);
    if (!lc) g[2] = v4u{0u, 0u, 0u, 0u};
    {
      const float gs = tr_sum16(asf4(g[0]), lane), us = tr_sum16(asf4(g[1]), lane), cs = tr_sum16(asf4(g[2]), lane);
      auto sw = __builtin_amdgcn_permlane16_swap(fbits(cs), fbits(cs), false, false);
      const bool ex = (lane & 3) == 1;
      const float gin = rbf(ex ? asf(sw[0]) : gs), uin = rbf(ex ? asf(sw[1]) : us);
      const float hv = rbf(silu_f(gin)) * uin;
      LAS unsigned* hs = (LAS unsigned*)(L + L_HS);
      if ((lane & 3) == 0 || (ex && lane < 16)) hs[ex ? 16 + vi : lane >> 2] = cvtpk(hv, hv);
    }
    WAIT_LGKM0();
    STAMP(3);
    wd_partial(c, poff8, *(const LAS v2u*)(L + L_HS + 8 * g8), ((const LAS unsigned*)(L + L_HS))[16 + (g8 & 3)], WD, sel, tag);
    if (lg < 18) {
      const float xr = ((const LAS float*)(L + L_XR))[vi];
      lds_wait(&flags[C_FP], 9u);
      STAMP(4);
      __builtin_amdgcn_s_sleep(DLY_F);
      const float fv = final_sum(rws, (unsigned)(WS_FP + rfin * 16), lane, tag);
      STAMP(5);
      const float gate = rbf(silu_f(rbf(fv)));
      lds_wait(&flags[F_S], 1u);
      const float sv = ((const LAS float*)(L + L_FIN))[vi];
      const float res = xr + rbf(rbf(sv) * gate);
      if (lane < 16 && (lane & 3) == 0) p.out[col] = (u16)cvtpk(res, res);
      if (b == 0 && lane == 0) __hip_atomic_store((GAS unsigned*)(p.ws + WS_EPOCH), flags[W_TAG], __ATOMIC_RELAXED, __HIP_MEMORY_SCOPE_AGENT);
      STAMP(6);
    }
  }
  } else {
  const int st = wave - 1;
  const Sel sel = mk_sel(lane);
  const rsrc_t rg = mk_rsrc((idx >> 4) ? p.wu : p.wg, D * F * 2);
  unsigned goff, goff4, ooff[2], doff[3];
  auto late_addr = [&]() {
    PIN();
    int ln = lane; asm volatile("" : "+v"(ln));
    const int g8_ = ln >> 3, c8_ = ln & 7;
    const unsigned gcol = (unsigned)(576 * xg + 64 * st + 8 * c8_);
    goff = (unsigned)(((36 * sp + 4 * g8_) * F + gcol) * 2);
    goff4 = g8_ < 4 ? (unsigned)(((36 * sp + 32 + g8_) * F + gcol) * 2) : OOB;
    wo_offsets(c, st, g8_, c8_, ooff);
    wd_offsets(c, st, g8_, c8_, doff);
    PIN();
  };
  const unsigned poff = strip_off(lane, st);
  v4u G[5];
  unsigned tn;
#define SLOT(dt) do { wait_until(tn); tn += (dt); } while (0)
  auto issue_g = [&](int t) { SLOT(DT2); G[t] = ld_nt(rg, t < 4 ? goff + t * (F * 2) : goff4); PIN(); };
  v4u gb[5];
  auto gu_frags = [&]() {
    const v4u x4 = *(const LAS v4u*)(L + L_XG + 16 * g8);
    const unsigned x5 = ((const LAS unsigned*)(L + L_XG))[32 + g8];
#pragma unroll
    for (int t = 0; t < 4; ++t) gb[t] = sel_b(x4[t], sel);
    gb[4] = sel_b(x5, sel);
    PIN();
  };
  auto do_gu = [&](const Tag& tag) {
    f32x4 acc = {0.f, 0.f, 0.f, 0.f};
#pragma unroll
    for (int t = 0; t < 5; ++t) acc = mfma(G[t], gb[t], acc);
    publish_at(c, poff + WS_GP + (xg * 32 + idx) * 2304, acc, tag);
    if (lane == 0) lds_add(&flags[C_GU]);
    PIN();
  };

  if (wave > 8) {
    tn = now32() + TSTART + 2 * DT1;
    late_addr();
#pragma unroll
    for (int t = 0; t < 5; ++t) { issue_g(t); tn += W9GAP - DT2; }
    lds_wait_slow(&flags[F_XN], 1u);
    const Tag tag = mk_tag(flags[W_TAG]);
    gu_frags();
    do_gu(tag);
    STAMP(2);
    lds_wait(&flags[C_ATT], 8u);
    STAMP(3);
    const int hh = g8 & 1;
    f32x2 ml[8]; f32x4 po[8];
#pragma unroll
    for (int w = 0; w < 8; ++w) { ml[w] = ((const LAS f32x2*)(L + L_ML))[w * 2 + hh]; po[w] = ((const LAS f32x4*)(L + L_PO))[w * 64 + lane]; }
    const float M = fmaxf(fmaxf(fmaxf(ml[0].x, ml[1].x), fmaxf(ml[2].x, ml[3].x)), fmaxf(fmaxf(ml[4].x, ml[5].x), fmaxf(ml[6].x, ml[7].x)));
    f32x4 o = {0.f, 0.f, 0.f, 0.f}; float Ls = 0.f;
#pragma unroll
    for (int w = 0; w < 8; ++w) {
      const float sc = __builtin_amdgcn_exp2f(ml[w].x - M);
      Ls = fmaf(sc, ml[w].y, Ls);
      o += sc * po[w];
    }
    const unsigned pbase = (unsigned)(WS_OP + (kvh * 16 + sp) * 1152);
    st_sc1(rws, pbase + (hh * 32 + (lane >> 4) * 8 + c8) * 16, pack4(o, tag));
    if ((lane & 0x37) == 0) st_sc1(rws, pbase + (64 + hh) * 16, pack2x(fbits(M), fbits(Ls), tag));
    if (lane == 0) flags[F_OP] = 1u;
    PIN();
    STAMP(4);
  } else {
    const size_t tile = ((size_t)kvh * SK + (SK - WIN)) * DH;
    const rsrc_t rk = mk_rsrc(p.kc + tile, WIN * DH * 2);
    const rsrc_t rv = mk_rsrc(p.vc + tile, WIN * DH * 2);
    const int cbase = (st * 8 * 16 + sp) * 1024;
    const unsigned koff = (unsigned)(((lane >> 2) & 3) * 256 + ((lane & 3) + 4 * (lane >> 4)) * 16);
    STAMP(13);
    tn = now32() + TSTART + (st * DT1) / 8;
    v4u K[8], V[8];
#define LDK(t) do { SLOT(DT1); if (t >= KWIN) WAIT_VM(KWIN - 1); K[t] = __builtin_amdgcn_raw_buffer_load_b128(rk, (int)koff, cbase + t * 16384, AUX_NT); PIN(); } while (0)
#define LDV(t) do { SLOT(DT1); WAIT_VM(KWIN - 1); V[t] = __builtin_amdgcn_raw_buffer_load_b128(rv, lane * 16, cbase + t * 16384, AUX_NT); PIN(); } while (0)
    unsigned shifted = 0;
#define CATCH_UP() do { const unsigned sh_ = UNI(flags[W_SHIFT]); tn -= sh_ - shifted; shifted = sh_; } while (0)
#pragma unroll
    for (int t = 0; t < 8; ++t) { LDK(t); if (t == 1) late_addr(); if (t == 5) CATCH_UP(); }
    constexpr float C2 = 0.08838834764831845f * 1.4426950408889634f;
    unsigned pk[8];
    auto qk_softmax = [&]() {
      const v4u qf = *(const LAS v4u*)(L + L_QB + ((g8 & 1) * 128 + ((lane & 3) + 4 * (lane >> 4)) * 8) * 2);
      float s[8];
#pragma unroll
      for (int t = 0; t < 8; ++t) {
        const f32x4 d = mfma(K[t], qf, f32x4{0.f, 0.f, 0.f, 0.f});
        s[t] = (dpp<0x00>(d[0]) + dpp<0x55>(d[1])) + (dpp<0xAA>(d[2]) + dpp<0xFF>(d[3]));
      }
      float m = fmaxf(fmaxf(fmaxf(s[0], s[1]), fmaxf(s[2], s[3])), fmaxf(fmaxf(s[4], s[5]), fmaxf(s[6], s[7])));
      m = x16_max(m) * C2;
      float l = 0.f;
#pragma unroll
      for (int t = 0; t < 8; ++t) { const float pr = __builtin_amdgcn_exp2f(fmaf(s[t], C2, -m)); l += pr; pk[t] = cvtpk(pr, pr); }
      l = x16_sum(l);
      if ((lane & 0x37) == 0) ((LAS f32x2*)(L + L_ML))[st * 2 + g8] = f32x2{m, l};
      PIN();
    };
    auto pv = [&]() {
      f32x4 oacc = {0.f, 0.f, 0.f, 0.f};
#pragma unroll
      for (int t = 0; t < 8; ++t) oacc = mfma(V[t], sel_b(pk[t], sel), oacc);
      ((LAS f32x4*)(L + L_PO))[st * 64 + lane] = oacc;
      WAIT_LGKM0();
      if (lane == 0) lds_add(&flags[C_ATT]);
      PIN();
    };
#pragma unroll
    for (int t = 0; t < 8; ++t) { LDV(t); if (t == 0 || t == 3) CATCH_UP(); }
    STAMP(11);
    tn -= (st * DT1) / 8;
    bool early = UNI(flags[F_Q]) != 0u;
    if (early) qk_softmax();
    STAMP(3);
    issue_g(0); issue_g(1);
    if (!early && UNI(flags[F_Q]) != 0u) { qk_softmax(); early = true; }
    STAMP(1);
    if (early) pv();
    STAMP(4);
    issue_g(2); issue_g(3); issue_g(4);
    if (!early) lds_wait(&flags[F_XN], 1u);
    const Tag tag = mk_tag(flags[W_TAG]);
    gu_frags();
    const rsrc_t ro = mk_rsrc(p.wo, NQ * D * 2);
    const rsrc_t rd = mk_rsrc(p.wd, F * D * 2);
    v4u WO[2], WD[3];
    f32x4 gacc = {0.f, 0.f, 0.f, 0.f};
#pragma unroll
    for (int t = 0; t < 3; ++t) gacc = mfma(G[t], gb[t], gacc);
    PIN();
    tn += DTD - DT2;
    SLOT(DTD); WD[0] = ld_nt(rd, doff[0]); PIN();
    STAMP(5);
    gacc = mfma(G[3], gb[3], gacc);
    gacc = mfma(G[4], gb[4], gacc);
    publish_at(c, poff + WS_GP + (xg * 32 + idx) * 2304, gacc, tag);
    if (lane == 0) lds_add(&flags[C_GU]);
    PIN();
    STAMP(6);
    if (!early) { lds_wait(&flags[F_Q], 1u); qk_softmax(); pv(); }
    STAMP(2);
    SLOT(DTD); WD[1] = ld_nt(rd, doff[1]); PIN();
    if (idx & 1) SLOT(DTD);
    WD[2] = ld_nt(rd, doff[2]); PIN();
    tn += DTO - DTD;
    SLOT(DT2); WO[0] = ld_nt(ro, ooff[0]); PIN();
    SLOT(DT2); WO[1] = ld_nt(ro, ooff[1]); PIN();
    v2u h2, o2; unsigned h3, spins = 0; bool hh;
#pragma nounroll
    for (;;) {
      h2 = *(const volatile LAS v2u*)(L + L_HS + 8 * g8); h3 = ((const volatile LAS unsigned*)(L + L_HS))[16 + (g8 & 3)];
      o2 = *(const volatile LAS v2u*)(L + L_OS + 8 * g8);
      hh = __all(h2.x != EMPTY && h2.y != EMPTY && h3 != EMPTY);
      if (hh || __all(o2.x != EMPTY && o2.y != EMPTY) || ++spins > (SPIN_CAP << 6)) break;
    }
    if (hh) {
      wd_partial(c, poff, h2, h3, WD, sel, tag);
      STAMP(8);
#pragma nounroll
      while (!__all(o2.x != EMPTY && o2.y != EMPTY) && ++spins <= (SPIN_CAP << 6)) o2 = *(const volatile LAS v2u*)(L + L_OS + 8 * g8);
      wo_partial(c, poff, o2, WO, sel, tag);
      STAMP(7);
    } else {
      wo_partial(c, poff, o2, WO, sel, tag);
      STAMP(7);
#pragma nounroll
      while (!__all(h2.x != EMPTY && h2.y != EMPTY && h3 != EMPTY) && ++spins <= (SPIN_CAP << 6)) {
        h2 = *(const volatile LAS v2u*)(L + L_HS + 8 * g8); h3 = ((const volatile LAS unsigned*)(L + L_HS))[16 + (g8 & 3)];
      }
      wd_partial(c, poff, h2, h3, WD, sel, tag);
      STAMP(8);
    }
  }
  }
  STAMP_FLUSH();
}

}

extern "C" void kernel_launch(void* const* d_in, const int* in_sizes, int n_in, void* d_out, int out_size, void* d_ws, size_t ws_size, hipStream_t stream) {
  Params p;
  p.x = (const u16*)d_in[0]; p.kc = (const u16*)d_in[1]; p.vc = (const u16*)d_in[2];
  p.wq = (const u16*)d_in[3]; p.wo = (const u16*)d_in[4];
  p.wg = (const u16*)d_in[6]; p.wu = (const u16*)d_in[7]; p.wd = (const u16*)d_in[8];
  p.out = (u16*)d_out; p.ws = (unsigned char*)d_ws;
  (void)hipFuncSetAttribute((const void*)fused_decode, hipFuncAttributeMaxDynamicSharedMemorySize, LDS_BYTES);
  hipLaunchKernelGGL(fused_decode, dim3(NWG), dim3(NTHR), LDS_BYTES, stream, p);
}
```

```cpp
#include <hip/hip_runtime.h>
#include <hip/hip_bf16.h>
#include <stdint.h>

#define GAS __attribute__((address_space(1)))
#define LAS __attribute__((address_space(3)))
typedef unsigned v4u __attribute__((ext_vector_type(4)));
typedef unsigned v2u __attribute__((ext_vector_type(2)));
typedef float f32x4 __attribute__((ext_vector_type(4)));
typedef float f32x2 __attribute__((ext_vector_type(2)));
typedef short bf16x8 __attribute__((ext_vector_type(8)));
typedef __bf16 bf16x2 __attribute__((ext_vector_type(2)));
typedef unsigned short u16;
typedef __amdgpu_buffer_rsrc_t rsrc_t;

#define KWIN 4
#define DT1 33
#define DT2 35
#define DTD 36
#define DTO 36
#define TSTART 10
#define W9GAP 70
#define T_Q 450
#define T_LAST 790
#define CODE_BYTES 18944
#define CODE_PF_WGS 2
#define TB_READ 170
#define TB_MAX 300
#define DLY_Q 24
#define DLY_O 30
#define DLY_H 20
#define DLY_F 20
#define DLY_S 28
#define RETRY 8

namespace {

constexpr int D = 576, SK = 65536, DH = 128, WIN = 4096, F = 4608, NQ = 4096;
constexpr int NWG = 256, NWAVE = 11, NTHR = NWAVE * 64;
constexpr int LDS_BYTES = 84 * 1024;

constexpr int WS_EPOCH = 0;
constexpr int WS_TB = 256;
constexpr int WS_QP = WS_TB + 1024;
constexpr int WS_OP = WS_QP + 16 * 4096;
constexpr int WS_GP = WS_OP + 16 * 16 * 1152;
constexpr int WS_SP = WS_GP + 8 * 32 * 2304;
constexpr int WS_FP = WS_SP + 256 * 2304;
constexpr int WS_END = WS_FP + 256 * 2304;

constexpr int L_FLAG = 0;
constexpr int L_OS = 64;
constexpr int L_HS = L_OS + 64;
constexpr int L_XQ = L_HS + 128;
constexpr int L_XG = L_XQ + 576;
constexpr int L_XR = L_XG + 160;
constexpr int L_QB = L_XR + 16;
constexpr int L_ML = L_QB + 512;
constexpr int L_FIN = L_ML + 128;
constexpr int L_PO = L_FIN + 16;
constexpr int L_X = L_PO + 8192;
constexpr int L_WQ = L_X + 2048;
constexpr int L_TS = L_WQ + 18 * 1024;
static_assert(L_PO % 16 == 0 && L_XQ % 16 == 0 && L_XG % 16 == 0 && L_QB % 16 == 0 && L_OS % 16 == 0 && L_HS % 16 == 0 && L_XR % 16 == 0 && L_X % 16 == 0, "align");
enum { F_XN = 0, F_Q = 1, C_ATT = 2, C_GU = 3, F_OP = 4, C_SP = 5, C_FP = 6, F_S = 7, W_TAG = 8, W_SHIFT = 9, F_X = 10, F_WQ = 11 };

constexpr int AUX_NT = 2, AUX_SC1 = 16;
constexpr unsigned VMASK = 0xFFFFFC00u, TMASK = 0x3FFu, OOB = 0xFFFFFF00u;
constexpr unsigned EMPTY = 0xFFFF0000u;
constexpr unsigned SPIN_CAP = 1u << 17;

struct Params {
  const u16* x; const u16* kc; const u16* vc; const u16* wq; const u16* wo;
  const u16* wg; const u16* wu; const u16* wd; u16* out; unsigned char* ws;
};

__device__ __forceinline__ unsigned fbits(float f) { return __builtin_bit_cast(unsigned, f); }
__device__ __forceinline__ float asf(unsigned u) { return __builtin_bit_cast(float, u); }
__device__ __forceinline__ unsigned cvtpk(float a, float b) { bf16x2 r = {(__bf16)a, (__bf16)b}; return __builtin_bit_cast(unsigned, r); }
__device__ __forceinline__ float rbf(float f) { return asf(cvtpk(0.f, f) & 0xffff0000u); }
__device__ __forceinline__ float silu_f(float x) { return x * __builtin_amdgcn_rcpf(1.f + __builtin_amdgcn_exp2f(x * -1.4426950408889634f)); }
__device__ __forceinline__ f32x4 mfma(v4u a, v4u b, f32x4 c) {
  return __builtin_amdgcn_mfma_f32_16x16x32_bf16(__builtin_bit_cast(bf16x8, a), __builtin_bit_cast(bf16x8, b), c, 0, 0, 0);
}
template <int CTRL> __device__ __forceinline__ float dpp(float x) {
  return asf(__builtin_amdgcn_mov_dpp(__builtin_bit_cast(int, x), CTRL, 0xf, 0xf, true));
}
template <int CTRL, int RM> __device__ __forceinline__ float dppm(float x) {
  return asf(__builtin_amdgcn_update_dpp(0, __builtin_bit_cast(int, x), CTRL, RM, 0xf, true));
}
constexpr int XOR1 = 0xB1, XOR2 = 0x4E, XOR7 = 0x141, ROR8 = 0x128;
__device__ __forceinline__ float x16_max(float x) {
  auto s = __builtin_amdgcn_permlane16_swap(fbits(x), fbits(x), false, false);
  x = fmaxf(asf(s[0]), asf(s[1]));
  auto t = __builtin_amdgcn_permlane32_swap(fbits(x), fbits(x), false, false);
  return fmaxf(asf(t[0]), asf(t[1]));
}
__device__ __forceinline__ float x16_sum(float x) {
  auto s = __builtin_amdgcn_permlane16_swap(fbits(x), fbits(x), false, false);
  x = asf(s[0]) + asf(s[1]);
  auto t = __builtin_amdgcn_permlane32_swap(fbits(x), fbits(x), false, false);
  return asf(t[0]) + asf(t[1]);
}
__device__ __forceinline__ float row_sum16(float x) { x += dpp<XOR1>(x); x += dpp<XOR2>(x); x += dpp<XOR7>(x); x += dpp<ROR8>(x); return x; }
__device__ __forceinline__ float row_max16(float x) { x = fmaxf(x, dpp<XOR1>(x)); x = fmaxf(x, dpp<XOR2>(x)); x = fmaxf(x, dpp<XOR7>(x)); x = fmaxf(x, dpp<ROR8>(x)); return x; }
__device__ __forceinline__ float wave_sum_s(float s) {
  s += dpp<0x111>(s); s += dpp<0x112>(s); s += dpp<0x114>(s); s += dpp<0x118>(s);
  s += dppm<0x142, 0xa>(s); s += dppm<0x143, 0xc>(s);
  return asf(__builtin_amdgcn_readlane(fbits(s), 63));
}
__device__ __forceinline__ float tr_sum16(f32x4 v, int lane) {
  const bool b3 = lane & 8, b2 = lane & 4;
  const float a = (b3 ? v[2] : v[0]) + dpp<ROR8>(b3 ? v[0] : v[2]);
  const float b = (b3 ? v[3] : v[1]) + dpp<ROR8>(b3 ? v[1] : v[3]);
  float r = (b2 ? b : a) + dpp<XOR7>(b2 ? a : b);
  r += dpp<XOR2>(r); r += dpp<XOR1>(r);
  return r;
}

__device__ __forceinline__ rsrc_t mk_rsrc(const void* p, unsigned bytes) {
  return __builtin_amdgcn_make_buffer_rsrc((void*)p, 0, (int)bytes, 0x00020000);
}
__device__ __forceinline__ v4u ld_nt(rsrc_t r, unsigned off) { return __builtin_amdgcn_raw_buffer_load_b128(r, (int)off, 0, AUX_NT); }
__device__ __forceinline__ v4u ld_sc1(rsrc_t r, unsigned off) { return __builtin_amdgcn_raw_buffer_load_b128(r, (int)off, 0, AUX_SC1); }
__device__ __forceinline__ void st_sc1(rsrc_t r, unsigned off, v4u g) { __builtin_amdgcn_raw_buffer_store_b128(g, r, (int)off, 0, AUX_SC1); }
__device__ __forceinline__ unsigned now32() { unsigned long long t; asm volatile("s_memrealtime %0\n\ts_waitcnt lgkmcnt(0)" : "=s"(t) :: "memory"); return (unsigned)t; }
__device__ __forceinline__ void wait_until(unsigned t) {
#pragma nounroll
  while ((int)(now32() - t) < 0) __builtin_amdgcn_s_sleep(1);
}
#define PIN() __builtin_amdgcn_sched_barrier(0)
#define CBAR() asm volatile("" ::: "memory")
#define WAIT_VM(n) asm volatile("s_waitcnt vmcnt(%0)" ::"i"(n) : "memory")
#define WAIT_LGKM0() asm volatile("s_waitcnt lgkmcnt(0)" ::: "memory")
#define UNI(x) __builtin_amdgcn_readfirstlane(x)

struct Tag { unsigned t0, t1, t2, t3; };
__device__ __forceinline__ Tag mk_tag(unsigned T) { return Tag{T & TMASK, (T + 0x155u) & TMASK, (T + 0x2AAu) & TMASK, (T + 0x0FFu) & TMASK}; }
__device__ __forceinline__ v4u pack4(f32x4 v, const Tag& t) {
  return v4u{(fbits(v.x) & VMASK) | t.t0, (fbits(v.y) & VMASK) | t.t1, (fbits(v.z) & VMASK) | t.t2, (fbits(v.w) & VMASK) | t.t3};
}
__device__ __forceinline__ v4u pack2x(unsigned a, unsigned b, const Tag& t) {
  return v4u{(a & VMASK) | t.t0, ((a & TMASK) << 10) | t.t1, (b & VMASK) | t.t2, ((b & TMASK) << 10) | t.t3};
}
__device__ __forceinline__ v4u untag(v4u g, const Tag& t) { return v4u{g.x ^ t.t0, g.y ^ t.t1, g.z ^ t.t2, g.w ^ t.t3}; }
__device__ __forceinline__ unsigned resid(v4u u) { return (u.x | u.y | u.z) | u.w; }
__device__ __forceinline__ f32x4 asf4(v4u u) { return __builtin_bit_cast(f32x4, u); }

template <int N>
__device__ __forceinline__ void poll(rsrc_t r, const unsigned (&off)[N], const bool (&live)[N], const Tag& tag, v4u (&res)[N]) {
  unsigned spins = 0;
#pragma nounroll
  for (;;) {
    v4u a[N];
#pragma unroll
    for (int i = 0; i < N; ++i) a[i] = ld_sc1(r, off[i]);
    CBAR();
    unsigned bad = 0;
#pragma unroll
    for (int i = 0; i < N; ++i) { res[i] = untag(a[i], tag); bad |= live[i] ? resid(res[i]) : 0u; }
    if (__all((bad & TMASK) == 0u) || ++spins > SPIN_CAP) break;
    __builtin_amdgcn_s_sleep(RETRY);
  }
}
__device__ __forceinline__ float final_sum(rsrc_t rws, unsigned base, int lane, const Tag& tag) {
  unsigned off[4]; bool live[4]; v4u g[4];
#pragma unroll
  for (int i = 0; i < 4; ++i) { off[i] = base + (unsigned)((i * 64 + lane) * 2304); live[i] = true; }
  poll<4>(rws, off, live, tag, g);
  const f32x4 v = (asf4(g[0]) + asf4(g[1])) + (asf4(g[2]) + asf4(g[3]));
  return x16_sum(tr_sum16(v, lane));
}

__device__ __forceinline__ void lds_wait(volatile LAS unsigned* f, unsigned want) {
  unsigned spins = 0;
#pragma nounroll
  while (*f < want) { if (++spins > (SPIN_CAP << 6)) break; }
  CBAR();
}
__device__ __forceinline__ void lds_wait_slow(volatile LAS unsigned* f, unsigned want) {
  unsigned spins = 0;
#pragma nounroll
  while (*f < want) { __builtin_amdgcn_s_sleep(2); if (++spins > (SPIN_CAP << 4)) break; }
  CBAR();
}
__device__ __forceinline__ void lds_add(volatile LAS unsigned* f) { __hip_atomic_fetch_add((LAS unsigned*)f, 1u, __ATOMIC_RELAXED, __HIP_MEMORY_SCOPE_WORKGROUP); }

struct Sel { unsigned m0, m1, m2, m3; };
__device__ __forceinline__ Sel mk_sel(int lane) {
  const unsigned h = (lane & 1) ? 0xFFFF0000u : 0x0000FFFFu; const int r = (lane & 7) >> 1;
  return Sel{r == 0 ? h : 0u, r == 1 ? h : 0u, r == 2 ? h : 0u, r == 3 ? h : 0u};
}
__device__ __forceinline__ v4u sel_b(unsigned dup, const Sel& s) { return v4u{dup & s.m0, dup & s.m1, dup & s.m2, dup & s.m3}; }
__device__ __forceinline__ f32x4 strip_fold(f32x4 d) {
  f32x4 y;
#pragma unroll
  for (int i = 0; i < 4; ++i) {
    const float t = dpp<ROR8>(d[i]);
    auto s = __builtin_amdgcn_permlane32_swap(fbits(t), fbits(t), false, false);
    y[i] = d[i] + asf(s[1]);
  }
  return y;
}

struct Ctx {
  LAS unsigned char* L; volatile LAS unsigned* flags; rsrc_t rws;
  int lane, g8, c8, b, xg, idx, kvh, sp;
};
__device__ __forceinline__ void wo_offsets(const Ctx& c, int st, int g8, int c8, unsigned (&off)[2]) {
  const int hrow = (2 * c.kvh + (c.sp >> 3)) * 128 + 32 * ((c.sp >> 1) & 3) + 4 * (c.sp & 1);
#pragma unroll
  for (int t = 0; t < 2; ++t) { const int u = 2 * g8 + t; off[t] = (unsigned)(((hrow + 8 * (u & 3) + (u >> 2)) * D + 64 * st + 8 * c8) * 2); }
}
__device__ __forceinline__ void wd_offsets(const Ctx& c, int st, int g8, int c8, unsigned (&off)[3]) {
  const int odd = c.idx & 1, g0 = 9 * (c.idx >> 1) + 4 * odd;
#pragma unroll
  for (int t = 0; t < 3; ++t) {
    const int u = t < 2 ? 2 * g8 + t : 16 + (g8 & 3), Gi = g0 + (u >> 2);
    const int row = 576 * c.xg + 64 * (Gi >> 4) + 32 * ((Gi >> 3) & 1) + 8 * (u & 3) + (Gi & 7);
    off[t] = (t < 2 || (odd && g8 < 4)) ? (unsigned)((row * D + 64 * st + 8 * c8) * 2) : OOB;
  }
}
__device__ __forceinline__ unsigned strip_off(int lane, int st) {
  unsigned o = (lane < 32 && (lane & 15) < 8) ? (unsigned)((st * 16 + (lane >> 4) * 8 + (lane & 7)) * 16) : 0x80000000u;
  asm volatile("" : "+v"(o));
  return o;
}
__device__ __forceinline__ void publish_at(const Ctx& c, unsigned off, f32x4 acc, const Tag& tag) {
  st_sc1(c.rws, off, pack4(strip_fold(acc), tag));
}
__device__ __forceinline__ void wo_partial(const Ctx& c, unsigned poff, v2u o2, const v4u (&WO)[2], const Sel& sel, const Tag& tag) {
  f32x4 acc = {0.f, 0.f, 0.f, 0.f};
  acc = mfma(WO[0], sel_b(o2.x, sel), acc);
  acc = mfma(WO[1], sel_b(o2.y, sel), acc);
  publish_at(c, poff + WS_SP + c.b * 2304, acc, tag);
  if (c.lane == 0) lds_add(&c.flags[C_SP]);
  PIN();
}
__device__ __forceinline__ void wd_partial(const Ctx& c, unsigned poff, v2u h2, unsigned h3, const v4u (&WD)[3], const Sel& sel, const Tag& tag) {
  f32x4 acc = {0.f, 0.f, 0.f, 0.f};
  acc = mfma(WD[0], sel_b(h2.x, sel), acc);
  acc = mfma(WD[1], sel_b(h2.y, sel), acc);
  acc = mfma(WD[2], sel_b(c.g8 < 4 ? h3 : 0u, sel), acc);
  publish_at(c, poff + WS_FP + c.b * 2304, acc, tag);
  if (c.lane == 0) lds_add(&c.flags[C_FP]);
  PIN();
}

#define STAMP(i) do {} while (0)
#define STAMP_INIT() do {} while (0)
#define STAMP_FLUSH() do {} while (0)

__global__ void __launch_bounds__(NTHR, 1) fused_decode(Params p) {
  extern __shared__ __attribute__((aligned(16))) unsigned char smem[];
  LAS unsigned char* L = (LAS unsigned char*)smem;
  volatile LAS unsigned* flags = (volatile LAS unsigned*)(L + L_FLAG);
  const int lane = threadIdx.x & 63;
  const int wave = UNI(threadIdx.x >> 6);
  const int b = blockIdx.x, xg = b & 7, idx = b >> 3;
  const int kvh = 2 * xg + (idx >> 4), sp = idx & 15;
  const rsrc_t rws = mk_rsrc(p.ws, WS_END);
  const int lg = (idx >> 3) * 8 + xg;
  const int rfin = lg * 8 + (idx & 7);
  const int g8 = lane >> 3, c8 = lane & 7;
  const Ctx c{L, flags, rws, lane, g8, c8, b, xg, idx, kvh, sp};

  unsigned long long code_base;
  asm volatile("s_getpc_b64 %0" : "=s"(code_base));
  code_base &= ~255ull;
  unsigned cpf0 = 0, cpf1 = 0, cpf2 = 0;
  if ((threadIdx.x >> 6) == 10 && (int)(blockIdx.x >> 3) < CODE_PF_WGS) {
    const GAS unsigned* cp = (const GAS unsigned*)code_base + (threadIdx.x & 63) * 32;
    if ((threadIdx.x & 63) * 128 + 16384 < CODE_BYTES) cpf2 = cp[4096];
    if ((threadIdx.x & 63) * 128 + 8192 < CODE_BYTES) cpf1 = cp[2048];
    cpf0 = cp[0];
  }
  PIN();
  STAMP_INIT();
  STAMP(0);
  const unsigned t_start = now32();
  asm volatile("" :: "s"(p.x), "s"(p.kc), "s"(p.vc), "s"(p.wq), "s"(p.wo), "s"(p.wg), "s"(p.wu), "s"(p.wd), "s"(p.out), "s"(p.ws));
  if (wave == 0 && lane < 52) flags[lane] = lane < 16 ? 0u : EMPTY;
  WAIT_LGKM0();
  __builtin_amdgcn_s_barrier();
  PIN();
  STAMP(12);

  if (wave == 10) {
    const unsigned E = __hip_atomic_load((const GAS unsigned*)(p.ws + WS_EPOCH), __ATOMIC_RELAXED, __HIP_MEMORY_SCOPE_AGENT);
    const rsrc_t rx = mk_rsrc(p.x, D * 2);
    __builtin_amdgcn_raw_ptr_buffer_load_lds(rx, (LAS void*)(L + L_X), 16, lane * 16, 0, 0, 0);
    __builtin_amdgcn_raw_ptr_buffer_load_lds(rx, (LAS void*)(L + L_X + 1024), 16, 1024 + lane * 16, 0, 0, 0);
    const rsrc_t rq = mk_rsrc(p.wq, D * NQ * 2);
    const unsigned qoff = (unsigned)(((144 * (sp >> 2) + 18 * g8) * NQ + kvh * 256 + 64 * (sp & 3) + 8 * c8) * 2);
#pragma unroll
    for (int t = 0; t < 18; ++t) __builtin_amdgcn_raw_ptr_buffer_load_lds(rq, (LAS void*)(L + L_WQ + 1024 * t), 16, qoff + t * (NQ * 2), 0, 0, AUX_NT);
    PIN();
    __builtin_amdgcn_raw_buffer_store_b16((unsigned short)0, mk_rsrc(p.out, D * 2),
                                          (lg < 18 && lane < 4) ? (64 * (rfin >> 4) + 32 * ((rfin >> 3) & 1) + 8 * lane + (rfin & 7)) * 2 : (int)OOB, 0, 0);
    PIN();
    STAMP(13);
    __builtin_amdgcn_s_waitcnt(0x4F73);
    const unsigned T = UNI(E) + 1u;
    asm volatile("ds_write_b32 %0, %1 offset:%2\n\ts_waitcnt lgkmcnt(0)\n\tds_write_b32 %0, %3 offset:%4" :: "v"(0u), "v"(T), "i"(L_FLAG + 4 * W_TAG), "v"(1u), "i"(L_FLAG + 4 * F_X) : "memory");
    st_sc1(rws, (idx == 0 && lane == 0) ? (unsigned)(WS_TB + xg * 128) : OOB, pack2x(t_start, 0u, mk_tag(T)));
    __builtin_amdgcn_s_waitcnt(0x0F72);
    asm volatile("ds_write_b32 %0, %1 offset:%2" :: "v"(0u), "v"(1u), "i"(L_FLAG + 4 * F_WQ) : "memory");
    STAMP(14);
  }
  unsigned wave2 = wave;
  asm volatile("" : "+s"(wave2));

  if (wave2 - 1u >= 9u) {
  if (wave2 == 0) {
    const int st = sp & 3, rs = sp >> 2;
    const Sel sel = mk_sel(lane);
    const unsigned qpoff = (lane < 32 && (lane & 15) < 8) ? (unsigned)(WS_QP + ((kvh * 4 + rs) * 4 + st) * 256 + ((lane >> 4) * 8 + c8) * 16) : OOB;
    lds_wait(&flags[F_X], 1u);
    const LAS unsigned* xl = (const LAS unsigned*)(L + L_X);
    const LAS u16* xh = (const LAS u16*)(L + L_X);
    unsigned xw[5];
#pragma unroll
    for (int i = 0; i < 4; ++i) xw[i] = xl[lane + 64 * i];
    xw[4] = lane < 32 ? xl[256 + lane] : 0u;
    const unsigned xq0 = xh[144 * rs + lane], xq1 = xh[144 * rs + 64 + lane], xq2 = xh[144 * rs + 128 + (lane & 15)];
    const unsigned xg0 = xh[36 * sp + (lane < 36 ? lane : 0)];
    const unsigned xr0 = xh[64 * (rfin >> 4) + 32 * ((rfin >> 3) & 1) + 8 * (lane & 3) + (rfin & 7)];
    const Tag tag = mk_tag(flags[W_TAG]);
    float xv[10];
#pragma unroll
    for (int i = 0; i < 5; ++i) { xv[2 * i] = asf(xw[i] << 16); xv[2 * i + 1] = asf(xw[i] & 0xffff0000u); }
    const float s = ((xv[0] + xv[1]) + (xv[2] + xv[3])) + ((xv[4] + xv[5]) + (xv[6] + xv[7])) + (xv[8] + xv[9]);
    const float mean = wave_sum_s(s) * (1.f / D);
    float q2 = 0.f, q3 = 0.f;
#pragma unroll
    for (int i = 0; i < 8; i += 2) { const float d0 = xv[i] - mean, d1 = xv[i + 1] - mean; q2 = fmaf(d0, d0, q2); q3 = fmaf(d1, d1, q3); }
    { const float d0 = lane < 32 ? xv[8] - mean : 0.f, d1 = lane < 32 ? xv[9] - mean : 0.f; q2 = fmaf(d0, d0, q2); q3 = fmaf(d1, d1, q3); }
    const float var = wave_sum_s(q2 + q3) * (1.f / D);
    const float mb = rbf(mean);
    const float den = rbf(__builtin_amdgcn_sqrtf(rbf(rbf(var) + 1e-5f)));
    const float rden = __builtin_amdgcn_rcpf(den);
    auto norm = [&](unsigned raw) -> unsigned {
      const float a = rbf(asf(raw << 16) - mb);
      float qv = a * rden; qv = fmaf(fmaf(-qv, den, a), rden, qv);
      return cvtpk(qv, qv);
    };
    LAS unsigned* xq = (LAS unsigned*)(L + L_XQ);
    xq[lane] = norm(xq0); xq[64 + lane] = norm(xq1);
    { const unsigned v2 = norm(xq2); if (lane < 16) xq[128 + lane] = v2; }
    WAIT_LGKM0();
    unsigned xv18[18];
    {
      const LAS unsigned* src = xq + 18 * g8;
#pragma unroll
      for (int i = 0; i < 9; ++i) { const v2u t2 = *(const LAS v2u*)(src + 2 * i); xv18[2 * i] = t2.x; xv18[2 * i + 1] = t2.y; }
    }
    STAMP(1);
    f32x4 acc = {0.f, 0.f, 0.f, 0.f};
    lds_wait(&flags[F_WQ], 1u);
#pragma unroll
    for (int t = 0; t < 18; ++t) acc = mfma(((const LAS v4u*)(L + L_WQ))[t * 64 + lane], sel_b(xv18[t], sel), acc);
    {
      const f32x4 y = strip_fold(acc);
      st_sc1(rws, qpoff, pack4(y, tag));
    }
    STAMP(2);
    { const unsigned vg = norm(xg0); if (lane < 40) ((LAS unsigned*)(L + L_XG))[lane] = lane < 36 ? vg : 0u; }
    if (lane < 4) ((LAS float*)(L + L_XR))[lane] = asf(xr0 << 16);
    WAIT_LGKM0();
    if (lane == 0) flags[F_XN] = 1u;
    {
      unsigned off[4]; bool live[4]; v4u g[4];
#pragma unroll
      for (int r = 0; r < 4; ++r) { off[r] = (unsigned)(WS_QP + (kvh * 4 + r) * 1024 + lane * 16); live[r] = true; }
      __builtin_amdgcn_s_sleep(DLY_Q);
      wait_until(t_start + T_Q - UNI(flags[W_SHIFT]));
      poll<4>(rws, off, live, tag, g);
      const f32x4 q4 = (asf4(g[0]) + asf4(g[1])) + (asf4(g[2]) + asf4(g[3]));
      LAS u16* qb = (LAS u16*)(L + L_QB) + 64 * (lane >> 4) + 32 * (g8 & 1) + c8;
      const unsigned p01 = cvtpk(q4[0], q4[1]), p23 = cvtpk(q4[2], q4[3]);
      qb[0] = (u16)p01; qb[8] = (u16)(p01 >> 16); qb[16] = (u16)p23; qb[24] = (u16)(p23 >> 16);
      WAIT_LGKM0();
      if (lane == 0) flags[F_Q] = 1u;
    }
    STAMP(3);
    unsigned ooff[2];
    wo_offsets(c, 8, g8, c8, ooff);
    const unsigned poff8 = strip_off(lane, 8);
    const rsrc_t ro = mk_rsrc(p.wo, NQ * D * 2);
    lds_wait_slow(&flags[F_OP], 1u);
    STAMP(4);
    v4u WO[2];
    WO[0] = ld_nt(ro, ooff[0]); WO[1] = ld_nt(ro, ooff[1]);
    PIN();
    {
      const unsigned pb = (unsigned)(WS_OP + (kvh * 16 + (lane & 15)) * 1152);
      unsigned off[2] = {pb + (4 * sp + (lane >> 4)) * 16, pb + (64 + (sp >> 3)) * 16};
      bool live[2] = {true, true}; v4u g[2];
      __builtin_amdgcn_s_sleep(DLY_O);
      poll<2>(rws, off, live, tag, g);
      const float m = asf(g[1].x | (g[1].y >> 10)), l = asf(g[1].z | (g[1].w >> 10));
      const float M = row_max16(m);
      const float wgt = __builtin_amdgcn_exp2f(m - M);
      const float Ls = row_sum16(wgt * l);
      const float o = tr_sum16(wgt * asf4(g[0]), lane) * __builtin_amdgcn_rcpf(Ls);
      if ((lane & 3) == 0) ((LAS unsigned*)(L + L_OS))[lane >> 2] = cvtpk(o, o);
      WAIT_LGKM0();
    }
    STAMP(5);
    wo_partial(c, poff8, *(const LAS v2u*)(L + L_OS + 8 * g8), WO, sel, tag);
    if (lg < 18) {
      lds_wait_slow(&flags[C_SP], 9u);
      __builtin_amdgcn_s_sleep(DLY_S);
      const float sv = final_sum(rws, (unsigned)(WS_SP + rfin * 16), lane, tag);
      if (lane < 16 && (lane & 3) == 0) ((LAS float*)(L + L_FIN))[lane >> 2] = sv;
      WAIT_LGKM0();
      if (lane == 0) flags[F_S] = 1u;
      STAMP(6);
    }
  } else {
    const Tag tag = mk_tag(flags[W_TAG]);
    int late;
    {
      wait_until(t_start + TB_READ);
      const v4u g = untag(ld_sc1(rws, lane < 8 ? (unsigned)(WS_TB + lane * 128) : OOB), tag);
      const bool ok = lane < 8 && (resid(g) & TMASK) == 0u;
      late = ok ? (int)(t_start - (g.x | (g.y >> 10))) : 0;
      late = late > TB_MAX ? 0 : late;
      late = max(late, __builtin_amdgcn_mov_dpp(late, XOR1, 0xf, 0xf, true));
      late = max(late, __builtin_amdgcn_mov_dpp(late, XOR2, 0xf, 0xf, true));
      late = max(late, __builtin_amdgcn_mov_dpp(late, XOR7, 0xf, 0xf, true));
      late = UNI(late);
      if (lane == 0 && late > 0) flags[W_SHIFT] = (unsigned)late;
    }
    asm volatile("" :: "v"(cpf0), "v"(cpf1), "v"(cpf2));
    const Sel sel = mk_sel(lane);
    const int g0 = 9 * (idx >> 1) + 4 * (idx & 1), odd = idx & 1;
    unsigned doff[3];
    wd_offsets(c, 8, g8, c8, doff);
    const unsigned poff8 = strip_off(lane, 8);
    const rsrc_t rd = mk_rsrc(p.wd, F * D * 2);
    const bool lc = odd && lane < 32;
    const unsigned pb = (unsigned)(WS_GP + (xg * 32 + (lane & 15)) * 2304);
    unsigned off[3] = {pb + (g0 + (lane >> 4)) * 16, pb + 16 * 2304 + (g0 + (lane >> 4)) * 16, lc ? pb + (lane >> 4) * (16 * 2304) + (g0 + 4) * 16 : OOB};
    bool live[3] = {true, true, lc}; v4u g[3];
    const int vi = (lane >> 2) & 3;
    const int col = 64 * (rfin >> 4) + 32 * ((rfin >> 3) & 1) + 8 * vi + (rfin & 7);
    wait_until(t_start + T_LAST - (unsigned)late);
    v4u WD[3];
#pragma unroll
    for (int t = 0; t < 3; ++t) WD[t] = ld_nt(rd, doff[t]);
    PIN();
    lds_wait(&flags[C_GU], 9u);
    STAMP(1);
    __builtin_amdgcn_s_sleep(DLY_H);
    poll<3>(rws, off, live, tag, g);
    STAMP(2);
    if (!lc) g[2] = v4u{0u, 0u, 0u, 0u};
    {
      const float gs = tr_sum16(asf4(g[0]), lane), us = tr_sum16(asf4(g[1]), lane), cs = tr_sum16(asf4(g[2]), lane);
      auto sw = __builtin_amdgcn_permlane16_swap(fbits(cs), fbits(cs), false, false);
      const bool ex = (lane & 3) == 1;
      const float gin = rbf(ex ? asf(sw[0]) : gs), uin = rbf(ex ? asf(sw[1]) : us);
      const float hv = rbf(silu_f(gin)) * uin;
      LAS unsigned* hs = (LAS unsigned*)(L + L_HS);
      if ((lane & 3) == 0 || (ex && lane < 16)) hs[ex ? 16 + vi : lane >> 2] = cvtpk(hv, hv);
    }
    WAIT_LGKM0();
    STAMP(3);
    wd_partial(c, poff8, *(const LAS v2u*)(L + L_HS + 8 * g8), ((const LAS unsigned*)(L + L_HS))[16 + (g8 & 3)], WD, sel, tag);
    if (lg < 18) {
      const float xr = ((const LAS float*)(L + L_XR))[vi];
      lds_wait(&flags[C_FP], 9u);
      STAMP(4);
      __builtin_amdgcn_s_sleep(DLY_F);
      const float fv = final_sum(rws, (unsigned)(WS_FP + rfin * 16), lane, tag);
      STAMP(5);
      const float gate = rbf(silu_f(rbf(fv)));
      lds_wait(&flags[F_S], 1u);
      const float sv = ((const LAS float*)(L + L_FIN))[vi];
      const float res = xr + rbf(rbf(sv) * gate);
      if (lane < 16 && (lane & 3) == 0) p.out[col] = (u16)cvtpk(res, res);
      if (b == 0 && lane == 0) __hip_atomic_store((GAS unsigned*)(p.ws + WS_EPOCH), flags[W_TAG], __ATOMIC_RELAXED, __HIP_MEMORY_SCOPE_AGENT);
      STAMP(6);
    }
  }
  } else {
  const int st = wave - 1;
  const Sel sel = mk_sel(lane);
  const rsrc_t rg = mk_rsrc((idx >> 4) ? p.wu : p.wg, D * F * 2);
  unsigned goff, goff4, ooff[2], doff[3];
  auto late_addr = [&]() {
    PIN();
    int ln = lane; asm volatile("" : "+v"(ln));
    const int g8_ = ln >> 3, c8_ = ln & 7;
    const unsigned gcol = (unsigned)(576 * xg + 64 * st + 8 * c8_);
    goff = (unsigned)(((36 * sp + 4 * g8_) * F + gcol) * 2);
    goff4 = g8_ < 4 ? (unsigned)(((36 * sp + 32 + g8_) * F + gcol) * 2) : OOB;
    wo_offsets(c, st, g8_, c8_, ooff);
    wd_offsets(c, st, g8_, c8_, doff);
    PIN();
  };
  const unsigned poff = strip_off(lane, st);
  v4u G[5];
  unsigned tn;
#define SLOT(dt) do { wait_until(tn); tn += (dt); } while (0)
  auto issue_g = [&](int t) { SLOT(DT2); G[t] = ld_nt(rg, t < 4 ? goff + t * (F * 2) : goff4); PIN(); };
  v4u gb[5];
  auto gu_frags = [&]() {
    const v4u x4 = *(const LAS v4u*)(L + L_XG + 16 * g8);
    const unsigned x5 = ((const LAS unsigned*)(L + L_XG))[32 + g8];
#pragma unroll
    for (int t = 0; t < 4; ++t) gb[t] = sel_b(x4[t], sel);
    gb[4] = sel_b(x5, sel);
    PIN();
  };
  auto do_gu = [&](const Tag& tag) {
    f32x4 acc = {0.f, 0.f, 0.f, 0.f};
#pragma unroll
    for (int t = 0; t < 5; ++t) acc = mfma(G[t], gb[t], acc);
    publish_at(c, poff + WS_GP + (xg * 32 + idx) * 2304, acc, tag);
    if (lane == 0) lds_add(&flags[C_GU]);
    PIN();
  };

  if (wave > 8) {
    tn = now32() + TSTART + 2 * DT1;
    late_addr();
#pragma unroll
    for (int t = 0; t < 5; ++t) { issue_g(t); tn += W9GAP - DT2; }
    lds_wait_slow(&flags[F_XN], 1u);
    const Tag tag = mk_tag(flags[W_TAG]);
    gu_frags();
    do_gu(tag);
    STAMP(2);
    lds_wait(&flags[C_ATT], 8u);
    STAMP(3);
    const int hh = g8 & 1;
    f32x2 ml[8]; f32x4 po[8];
#pragma unroll
    for (int w = 0; w < 8; ++w) { ml[w] = ((const LAS f32x2*)(L + L_ML))[w * 2 + hh]; po[w] = ((const LAS f32x4*)(L + L_PO))[w * 64 + lane]; }
    const float M = fmaxf(fmaxf(fmaxf(ml[0].x, ml[1].x), fmaxf(ml[2].x, ml[3].x)), fmaxf(fmaxf(ml[4].x, ml[5].x), fmaxf(ml[6].x, ml[7].x)));
    f32x4 o = {0.f, 0.f, 0.f, 0.f}; float Ls = 0.f;
#pragma unroll
    for (int w = 0; w < 8; ++w) {
      const float sc = __builtin_amdgcn_exp2f(ml[w].x - M);
      Ls = fmaf(sc, ml[w].y, Ls);
      o += sc * po[w];
    }
    const unsigned pbase = (unsigned)(WS_OP + (kvh * 16 + sp) * 1152);
    st_sc1(rws, pbase + (hh * 32 + (lane >> 4) * 8 + c8) * 16, pack4(o, tag));
    if ((lane & 0x37) == 0) st_sc1(rws, pbase + (64 + hh) * 16, pack2x(fbits(M), fbits(Ls), tag));
    if (lane == 0) flags[F_OP] = 1u;
    PIN();
    STAMP(4);
  } else {
    const size_t tile = ((size_t)kvh * SK + (SK - WIN)) * DH;
    const rsrc_t rk = mk_rsrc(p.kc + tile, WIN * DH * 2);
    const rsrc_t rv = mk_rsrc(p.vc + tile, WIN * DH * 2);
    const int cbase = (st * 8 * 16 + sp) * 1024;
    const unsigned koff = (unsigned)(((lane >> 2) & 3) * 256 + ((lane & 3) + 4 * (lane >> 4)) * 16);
    STAMP(13);
    tn = now32() + TSTART + (st * DT1) / 8;
    v4u K[8], V[8];
#define LDK(t) do { SLOT(DT1); if (t >= KWIN) WAIT_VM(KWIN - 1); K[t] = __builtin_amdgcn_raw_buffer_load_b128(rk, (int)koff, cbase + t * 16384, AUX_NT); PIN(); } while (0)
#define LDV(t) do { SLOT(DT1); WAIT_VM(KWIN - 1); V[t] = __builtin_amdgcn_raw_buffer_load_b128(rv, lane * 16, cbase + t * 16384, AUX_NT); PIN(); } while (0)
    unsigned shifted = 0;
#define CATCH_UP() do { const unsigned sh_ = UNI(flags[W_SHIFT]); tn -= sh_ - shifted; shifted = sh_; } while (0)
#pragma unroll
    for (int t = 0; t < 8; ++t) { LDK(t); if (t == 1) late_addr(); if (t == 5) CATCH_UP(); }
    constexpr float C2 = 0.08838834764831845f * 1.4426950408889634f;
    unsigned pk[8];
    auto qk_softmax = [&]() {
      const v4u qf = *(const LAS v4u*)(L + L_QB + ((g8 & 1) * 128 + ((lane & 3) + 4 * (lane >> 4)) * 8) * 2);
      float s[8];
#pragma unroll
      for (int t = 0; t < 8; ++t) {
        const f32x4 d = mfma(K[t], qf, f32x4{0.f, 0.f, 0.f, 0.f});
        s[t] = (dpp<0x00>(d[0]) + dpp<0x55>(d[1])) + (dpp<0xAA>(d[2]) + dpp<0xFF>(d[3]));
      }
      float m = fmaxf(fmaxf(fmaxf(s[0], s[1]), fmaxf(s[2], s[3])), fmaxf(fmaxf(s[4], s[5]), fmaxf(s[6], s[7])));
      m = x16_max(m) * C2;
      float l = 0.f;
#pragma unroll
      for (int t = 0; t < 8; ++t) { const float pr = __builtin_amdgcn_exp2f(fmaf(s[t], C2, -m)); l += pr; pk[t] = cvtpk(pr, pr); }
      l = x16_sum(l);
      if ((lane & 0x37) == 0) ((LAS f32x2*)(L + L_ML))[st * 2 + g8] = f32x2{m, l};
      PIN();
    };
    auto pv = [&]() {
      f32x4 oacc = {0.f, 0.f, 0.f, 0.f};
#pragma unroll
      for (int t = 0; t < 8; ++t) oacc = mfma(V[t], sel_b(pk[t], sel), oacc);
      ((LAS f32x4*)(L + L_PO))[st * 64 + lane] = oacc;
      WAIT_LGKM0();
      if (lane == 0) lds_add(&flags[C_ATT]);
      PIN();
    };
#pragma unroll
    for (int t = 0; t < 8; ++t) { LDV(t); if (t == 0 || t == 3) CATCH_UP(); }
    STAMP(11);
    tn -= (st * DT1) / 8;
    bool early = UNI(flags[F_Q]) != 0u;
    if (early) qk_softmax();
    STAMP(3);
    issue_g(0); issue_g(1);
    if (!early && UNI(flags[F_Q]) != 0u) { qk_softmax(); early = true; }
    STAMP(1);
    if (early) pv();
    STAMP(4);
    issue_g(2); issue_g(3); issue_g(4);
    if (!early) lds_wait(&flags[F_XN], 1u);
    const Tag tag = mk_tag(flags[W_TAG]);
    gu_frags();
    const rsrc_t ro = mk_rsrc(p.wo, NQ * D * 2);
    const rsrc_t rd = mk_rsrc(p.wd, F * D * 2);
    v4u WO[2], WD[3];
    f32x4 gacc = {0.f, 0.f, 0.f, 0.f};
#pragma unroll
    for (int t = 0; t < 3; ++t) gacc = mfma(G[t], gb[t], gacc);
    PIN();
    tn += DTD - DT2;
    SLOT(DTD); WD[0] = ld_nt(rd, doff[0]); PIN();
    STAMP(5);
    gacc = mfma(G[3], gb[3], gacc);
    gacc = mfma(G[4], gb[4], gacc);
    publish_at(c, poff + WS_GP + (xg * 32 + idx) * 2304, gacc, tag);
    if (lane == 0) lds_add(&flags[C_GU]);
    PIN();
    STAMP(6);
    if (!early) { lds_wait(&flags[F_Q], 1u); qk_softmax(); pv(); }
    STAMP(2);
    SLOT(DTD); WD[1] = ld_nt(rd, doff[1]); PIN();
    if (idx & 1) SLOT(DTD);
    WD[2] = ld_nt(rd, doff[2]); PIN();
    tn += DTO - DTD;
    SLOT(DT2); WO[0] = ld_nt(ro, ooff[0]); PIN();
    SLOT(DT2); WO[1] = ld_nt(ro, ooff[1]); PIN();
    v2u h2, o2; unsigned h3, spins = 0; bool hh;
#pragma nounroll
    for (;;) {
      h2 = *(const volatile LAS v2u*)(L + L_HS + 8 * g8); h3 = ((const volatile LAS unsigned*)(L + L_HS))[16 + (g8 & 3)];
      o2 = *(const volatile LAS v2u*)(L + L_OS + 8 * g8);
      hh = __all(h2.x != EMPTY && h2.y != EMPTY && h3 != EMPTY);
      if (hh || __all(o2.x != EMPTY && o2.y != EMPTY) || ++spins > (SPIN_CAP << 6)) break;
    }
    if (hh) {
      wd_partial(c, poff, h2, h3, WD, sel, tag);
      STAMP(8);
#pragma nounroll
      while (!__all(o2.x != EMPTY && o2.y != EMPTY) && ++spins <= (SPIN_CAP << 6)) o2 = *(const volatile LAS v2u*)(L + L_OS + 8 * g8);
      wo_partial(c, poff, o2, WO, sel, tag);
      STAMP(7);
    } else {
      wo_partial(c, poff, o2, WO, sel, tag);
      STAMP(7);
#pragma nounroll
      while (!__all(h2.x != EMPTY && h2.y != EMPTY && h3 != EMPTY) && ++spins <= (SPIN_CAP << 6)) {
        h2 = *(const volatile LAS v2u*)(L + L_HS + 8 * g8); h3 = ((const volatile LAS unsigned*)(L + L_HS))[16 + (g8 & 3)];
      }
      wd_partial(c, poff, h2, h3, WD, sel, tag);
      STAMP(8);
    }
  }
  }
  STAMP_FLUSH();
}

}

extern "C" void kernel_launch(void* const* d_in, const int* in_sizes, int n_in, void* d_out, int out_size, void* d_ws, size_t ws_size, hipStream_t stream) {
  Params p;
  p.x = (const u16*)d_in[0]; p.kc = (const u16*)d_in[1]; p.vc = (const u16*)d_in[2];
  p.wq = (const u16*)d_in[3]; p.wo = (const u16*)d_in[4];
  p.wg = (const u16*)d_in[6]; p.wu = (const u16*)d_in[7]; p.wd = (const u16*)d_in[8];
  p.out = (u16*)d_out; p.ws = (unsigned char*)d_ws;
  (void)hipFuncSetAttribute((const void*)fused_decode, hipFuncAttributeMaxDynamicSharedMemorySize, LDS_BYTES);
  hipLaunchKernelGGL(fused_decode, dim3(NWG), dim3(NTHR), LDS_BYTES, stream, p);
}
```

```cpp
#include <hip/hip_runtime.h>
#include <hip/hip_bf16.h>
#include <stdint.h>

#define GAS __attribute__((address_space(1)))
#define LAS __attribute__((address_space(3)))
typedef unsigned v4u __attribute__((ext_vector_type(4)));
typedef unsigned v2u __attribute__((ext_vector_type(2)));
typedef float f32x4 __attribute__((ext_vector_type(4)));
typedef float f32x2 __attribute__((ext_vector_type(2)));
typedef short bf16x8 __attribute__((ext_vector_type(8)));
typedef __bf16 bf16x2 __attribute__((ext_vector_type(2)));
typedef unsigned short u16;
typedef __amdgpu_buffer_rsrc_t rsrc_t;

namespace {

constexpr int KWIN = 4;
constexpr int DT1 = 33;
constexpr int DT2 = 33;
constexpr int DTD = 24;
constexpr int DTO = 45;
constexpr int TSTART = 10;
constexpr int W9GAP = 70;
constexpr int T_Q = 450;
constexpr int T_LAST = 770;
constexpr int CODE_BYTES = 18944;
constexpr int CODE_PF_WGS = 2;
constexpr int TB_READ = 170;
constexpr int TB_MAX = 300;
constexpr int DLY_Q = 24;
constexpr int DLY_O = 30;
constexpr int DLY_H = 22;
constexpr int DLY_F = 22;
constexpr int DLY_S = 28;
constexpr int RETRY = 8;

constexpr int D = 576, SK = 65536, DH = 128, WIN = 4096, F = 4608, NQ = 4096;
constexpr int NWG = 256, NWAVE = 11, NTHR = NWAVE * 64;
constexpr int LDS_BYTES = 84 * 1024;

constexpr int WS_EPOCH = 0;
constexpr int WS_TB = 256;
constexpr int WS_QP = WS_TB + 1024;
constexpr int WS_OP = WS_QP + 16 * 4096;
constexpr int WS_GP = WS_OP + 16 * 16 * 1152;
constexpr int WS_SP = WS_GP + 8 * 32 * 2304;
constexpr int WS_FP = WS_SP + 256 * 2304;
constexpr int WS_END = WS_FP + 256 * 2304;

constexpr int L_FLAG = 0;
constexpr int L_OS = 64;
constexpr int L_HS = L_OS + 64;
constexpr int L_XQ = L_HS + 128;
constexpr int L_XG = L_XQ + 576;
constexpr int L_XR = L_XG + 160;
constexpr int L_QB = L_XR + 16;
constexpr int L_ML = L_QB + 512;
constexpr int L_FIN = L_ML + 128;
constexpr int L_PO = L_FIN + 16;
constexpr int L_X = L_PO + 8192;
constexpr int L_WQ = L_X + 2048;
static_assert(L_PO % 16 == 0 && L_XQ % 16 == 0 && L_XG % 16 == 0 && L_QB % 16 == 0 && L_OS % 16 == 0 && L_HS % 16 == 0 && L_XR % 16 == 0 && L_X % 16 == 0, "align");
enum { F_XN = 0, F_Q = 1, C_ATT = 2, C_GU = 3, F_OP = 4, C_SP = 5, C_FP = 6, F_S = 7, W_TAG = 8, W_SHIFT = 9, F_X = 10, F_WQ = 11 };

constexpr int AUX_NT = 2, AUX_SC1 = 16;
constexpr unsigned VMASK = 0xFFFFFC00u, TMASK = 0x3FFu, OOB = 0xFFFFFF00u;
constexpr unsigned EMPTY = 0xFFFF0000u;
constexpr unsigned SPIN_CAP = 1u << 17;

struct Params {
  const u16* x; const u16* kc; const u16* vc; const u16* wq; const u16* wo;
  const u16* wg; const u16* wu; const u16* wd; u16* out; unsigned char* ws;
};

__device__ __forceinline__ unsigned fbits(float f) { return __builtin_bit_cast(unsigned, f); }
__device__ __forceinline__ float asf(unsigned u) { return __builtin_bit_cast(float, u); }
__device__ __forceinline__ unsigned cvtpk(float a, float b) { bf16x2 r = {(__bf16)a, (__bf16)b}; return __builtin_bit_cast(unsigned, r); }
__device__ __forceinline__ float rbf(float f) { return asf(cvtpk(0.f, f) & 0xffff0000u); }
__device__ __forceinline__ float silu_f(float x) { return x * __builtin_amdgcn_rcpf(1.f + __builtin_amdgcn_exp2f(x * -1.4426950408889634f)); }
__device__ __forceinline__ f32x4 mfma(v4u a, v4u b, f32x4 c) {
  return __builtin_amdgcn_mfma_f32_16x16x32_bf16(__builtin_bit_cast(bf16x8, a), __builtin_bit_cast(bf16x8, b), c, 0, 0, 0);
}
template <int CTRL> __device__ __forceinline__ float dpp(float x) {
  return asf(__builtin_amdgcn_mov_dpp(__builtin_bit_cast(int, x), CTRL, 0xf, 0xf, true));
}
template <int CTRL, int RM> __device__ __forceinline__ float dppm(float x) {
  return asf(__builtin_amdgcn_update_dpp(0, __builtin_bit_cast(int, x), CTRL, RM, 0xf, true));
}
constexpr int XOR1 = 0xB1, XOR2 = 0x4E, XOR7 = 0x141, ROR8 = 0x128;
__device__ __forceinline__ float x16_max(float x) {
  auto s = __builtin_amdgcn_permlane16_swap(fbits(x), fbits(x), false, false);
  x = fmaxf(asf(s[0]), asf(s[1]));
  auto t = __builtin_amdgcn_permlane32_swap(fbits(x), fbits(x), false, false);
  return fmaxf(asf(t[0]), asf(t[1]));
}
__device__ __forceinline__ float x16_sum(float x) {
  auto s = __builtin_amdgcn_permlane16_swap(fbits(x), fbits(x), false, false);
  x = asf(s[0]) + asf(s[1]);
  auto t = __builtin_amdgcn_permlane32_swap(fbits(x), fbits(x), false, false);
  return asf(t[0]) + asf(t[1]);
}
__device__ __forceinline__ float row_sum16(float x) { x += dpp<XOR1>(x); x += dpp<XOR2>(x); x += dpp<XOR7>(x); x += dpp<ROR8>(x); return x; }
__device__ __forceinline__ float row_max16(float x) { x = fmaxf(x, dpp<XOR1>(x)); x = fmaxf(x, dpp<XOR2>(x)); x = fmaxf(x, dpp<XOR7>(x)); x = fmaxf(x, dpp<ROR8>(x)); return x; }
__device__ __forceinline__ float wave_sum_s(float s) {
  s += dpp<0x111>(s); s += dpp<0x112>(s); s += dpp<0x114>(s); s += dpp<0x118>(s);
  s += dppm<0x142, 0xa>(s); s += dppm<0x143, 0xc>(s);
  return asf(__builtin_amdgcn_readlane(fbits(s), 63));
}
__device__ __forceinline__ float tr_sum16(f32x4 v, int lane) {
  const bool b3 = lane & 8, b2 = lane & 4;
  const float a = (b3 ? v[2] : v[0]) + dpp<ROR8>(b3 ? v[0] : v[2]);
  const float b = (b3 ? v[3] : v[1]) + dpp<ROR8>(b3 ? v[1] : v[3]);
  float r = (b2 ? b : a) + dpp<XOR7>(b2 ? a : b);
  r += dpp<XOR2>(r); r += dpp<XOR1>(r);
  return r;
}

__device__ __forceinline__ rsrc_t mk_rsrc(const void* p, unsigned bytes) {
  return __builtin_amdgcn_make_buffer_rsrc((void*)p, 0, (int)bytes, 0x00020000);
}
__device__ __forceinline__ v4u ld_nt(rsrc_t r, unsigned off) { return __builtin_amdgcn_raw_buffer_load_b128(r, (int)off, 0, AUX_NT); }
__device__ __forceinline__ v4u ld_sc1(rsrc_t r, unsigned off) { return __builtin_amdgcn_raw_buffer_load_b128(r, (int)off, 0, AUX_SC1); }
__device__ __forceinline__ void st_sc1(rsrc_t r, unsigned off, v4u g) { __builtin_amdgcn_raw_buffer_store_b128(g, r, (int)off, 0, AUX_SC1); }
__device__ __forceinline__ unsigned now32() { unsigned long long t; asm volatile("s_memrealtime %0\n\ts_waitcnt lgkmcnt(0)" : "=s"(t) :: "memory"); return (unsigned)t; }
__device__ __forceinline__ void wait_until(unsigned t) {
#pragma nounroll
  while ((int)(now32() - t) < 0) __builtin_amdgcn_s_sleep(1);
}
#define PIN() __builtin_amdgcn_sched_barrier(0)
#define CBAR() asm volatile("" ::: "memory")
#define WAIT_VM(n) asm volatile("s_waitcnt vmcnt(%0)" ::"i"(n) : "memory")
#define WAIT_LGKM0() asm volatile("s_waitcnt lgkmcnt(0)" ::: "memory")
#define UNI(x) __builtin_amdgcn_readfirstlane(x)

struct Tag { unsigned t0, t1, t2, t3; };
__device__ __forceinline__ Tag mk_tag(unsigned T) { return Tag{T & TMASK, (T + 0x155u) & TMASK, (T + 0x2AAu) & TMASK, (T + 0x0FFu) & TMASK}; }
__device__ __forceinline__ v4u pack4(f32x4 v, const Tag& t) {
  return v4u{(fbits(v.x) & VMASK) | t.t0, (fbits(v.y) & VMASK) | t.t1, (fbits(v.z) & VMASK) | t.t2, (fbits(v.w) & VMASK) | t.t3};
}
__device__ __forceinline__ v4u pack2x(unsigned a, unsigned b, const Tag& t) {
  return v4u{(a & VMASK) | t.t0, ((a & TMASK) << 10) | t.t1, (b & VMASK) | t.t2, ((b & TMASK) << 10) | t.t3};
}
__device__ __forceinline__ v4u untag(v4u g, const Tag& t) { return v4u{g.x ^ t.t0, g.y ^ t.t1, g.z ^ t.t2, g.w ^ t.t3}; }
__device__ __forceinline__ unsigned resid(v4u u) { return (u.x | u.y | u.z) | u.w; }
__device__ __forceinline__ f32x4 asf4(v4u u) { return __builtin_bit_cast(f32x4, u); }

template <int N>
__device__ __forceinline__ void poll(rsrc_t r, const unsigned (&off)[N], const bool (&live)[N], const Tag& tag, v4u (&res)[N]) {
  unsigned spins = 0;
#pragma nounroll
  for (;;) {
    v4u a[N];
#pragma unroll
    for (int i = 0; i < N; ++i) a[i] = ld_sc1(r, off[i]);
    CBAR();
    unsigned bad = 0;
#pragma unroll
    for (int i = 0; i < N; ++i) { res[i] = untag(a[i], tag); bad |= live[i] ? resid(res[i]) : 0u; }
    if (__all((bad & TMASK) == 0u) || ++spins > SPIN_CAP) break;
    __builtin_amdgcn_s_sleep(RETRY);
  }
}
__device__ __forceinline__ float final_sum(rsrc_t rws, unsigned base, int lane, const Tag& tag) {
  unsigned off[4]; bool live[4]; v4u g[4];
#pragma unroll
  for (int i = 0; i < 4; ++i) { off[i] = base + (unsigned)((i * 64 + lane) * 2304); live[i] = true; }
  poll<4>(rws, off, live, tag, g);
  const f32x4 v = (asf4(g[0]) + asf4(g[1])) + (asf4(g[2]) + asf4(g[3]));
  return x16_sum(tr_sum16(v, lane));
}

__device__ __forceinline__ void lds_wait(volatile LAS unsigned* f, unsigned want) {
  unsigned spins = 0;
#pragma nounroll
  while (*f < want) { if (++spins > (SPIN_CAP << 6)) break; }
  CBAR();
}
__device__ __forceinline__ void lds_wait_slow(volatile LAS unsigned* f, unsigned want) {
  unsigned spins = 0;
#pragma nounroll
  while (*f < want) { __builtin_amdgcn_s_sleep(2); if (++spins > (SPIN_CAP << 4)) break; }
  CBAR();
}
__device__ __forceinline__ void lds_add(volatile LAS unsigned* f) { __hip_atomic_fetch_add((LAS unsigned*)f, 1u, __ATOMIC_RELAXED, __HIP_MEMORY_SCOPE_WORKGROUP); }

struct Sel { unsigned m0, m1, m2, m3; };
__device__ __forceinline__ Sel mk_sel(int lane) {
  const unsigned h = (lane & 1) ? 0xFFFF0000u : 0x0000FFFFu; const int r = (lane & 7) >> 1;
  return Sel{r == 0 ? h : 0u, r == 1 ? h : 0u, r == 2 ? h : 0u, r == 3 ? h : 0u};
}
__device__ __forceinline__ v4u sel_b(unsigned dup, const Sel& s) { return v4u{dup & s.m0, dup & s.m1, dup & s.m2, dup & s.m3}; }
__device__ __forceinline__ f32x4 strip_fold(f32x4 d) {
  f32x4 y;
#pragma unroll
  for (int i = 0; i < 4; ++i) {
    const float t = dpp<ROR8>(d[i]);
    auto s = __builtin_amdgcn_permlane32_swap(fbits(t), fbits(t), false, false);
    y[i] = d[i] + asf(s[1]);
  }
  return y;
}

struct Ctx {
  LAS unsigned char* L; volatile LAS unsigned* flags; rsrc_t rws;
  int lane, g8, c8, b, xg, idx, kvh, sp;
};
__device__ __forceinline__ void wo_offsets(const Ctx& c, int st, int g8, int c8, unsigned (&off)[2]) {
  const int hrow = (2 * c.kvh + (c.sp >> 3)) * 128 + 32 * ((c.sp >> 1) & 3) + 4 * (c.sp & 1);
#pragma unroll
  for (int t = 0; t < 2; ++t) { const int u = 2 * g8 + t; off[t] = (unsigned)(((hrow + 8 * (u & 3) + (u >> 2)) * D + 64 * st + 8 * c8) * 2); }
}
__device__ __forceinline__ void wd_offsets(const Ctx& c, int st, int g8, int c8, unsigned (&off)[3]) {
  const int odd = c.idx & 1, g0 = 9 * (c.idx >> 1) + 4 * odd;
#pragma unroll
  for (int t = 0; t < 3; ++t) {
    const int u = t < 2 ? 2 * g8 + t : 16 + (g8 & 3), Gi = g0 + (u >> 2);
    const int row = 576 * c.xg + 64 * (Gi >> 4) + 32 * ((Gi >> 3) & 1) + 8 * (u & 3) + (Gi & 7);
    off[t] = (t < 2 || (odd && g8 < 4)) ? (unsigned)((row * D + 64 * st + 8 * c8) * 2) : OOB;
  }
}
__device__ __forceinline__ unsigned strip_off(int lane, int st) {
  unsigned o = (lane < 32 && (lane & 15) < 8) ? (unsigned)((st * 16 + (lane >> 4) * 8 + (lane & 7)) * 16) : 0x80000000u;
  asm volatile("" : "+v"(o));
  return o;
}
__device__ __forceinline__ void publish_at(const Ctx& c, unsigned off, f32x4 acc, const Tag& tag) {
  st_sc1(c.rws, off, pack4(strip_fold(acc), tag));
}
__device__ __forceinline__ void wo_partial(const Ctx& c, unsigned poff, v2u o2, const v4u (&WO)[2], const Sel& sel, const Tag& tag) {
  f32x4 acc = {0.f, 0.f, 0.f, 0.f};
  acc = mfma(WO[0], sel_b(o2.x, sel), acc);
  acc = mfma(WO[1], sel_b(o2.y, sel), acc);
  publish_at(c, poff + WS_SP + c.b * 2304, acc, tag);
  if (c.lane == 0) lds_add(&c.flags[C_SP]);
  PIN();
}
__device__ __forceinline__ void wd_partial(const Ctx& c, unsigned poff, v2u h2, unsigned h3, const v4u (&WD)[3], const Sel& sel, const Tag& tag) {
  f32x4 acc = {0.f, 0.f, 0.f, 0.f};
  acc = mfma(WD[0], sel_b(h2.x, sel), acc);
  acc = mfma(WD[1], sel_b(h2.y, sel), acc);
  acc = mfma(WD[2], sel_b(c.g8 < 4 ? h3 : 0u, sel), acc);
  publish_at(c, poff + WS_FP + c.b * 2304, acc, tag);
  if (c.lane == 0) lds_add(&c.flags[C_FP]);
  PIN();
}

__global__ void __launch_bounds__(NTHR, 1) fused_decode(Params p) {
  extern __shared__ __attribute__((aligned(16))) unsigned char smem[];
  LAS unsigned char* L = (LAS unsigned char*)smem;
  volatile LAS unsigned* flags = (volatile LAS unsigned*)(L + L_FLAG);
  const int lane = threadIdx.x & 63;
  const int wave = UNI(threadIdx.x >> 6);
  const int b = blockIdx.x, xg = b & 7, idx = b >> 3;
  const int kvh = 2 * xg + (idx >> 4), sp = idx & 15;
  const rsrc_t rws = mk_rsrc(p.ws, WS_END);
  const int lg = (idx >> 3) * 8 + xg;
  const int rfin = lg * 8 + (idx & 7);
  const int g8 = lane >> 3, c8 = lane & 7;
  const Ctx c{L, flags, rws, lane, g8, c8, b, xg, idx, kvh, sp};

  unsigned long long code_base;
  asm volatile("s_getpc_b64 %0" : "=s"(code_base));
  code_base &= ~255ull;
  unsigned cpf0 = 0, cpf1 = 0, cpf2 = 0;
  if ((threadIdx.x >> 6) == 10 && (int)(blockIdx.x >> 3) < CODE_PF_WGS) {
    const GAS unsigned* cp = (const GAS unsigned*)code_base + (threadIdx.x & 63) * 32;
    if ((threadIdx.x & 63) * 128 + 16384 < CODE_BYTES) cpf2 = cp[4096];
    if ((threadIdx.x & 63) * 128 + 8192 < CODE_BYTES) cpf1 = cp[2048];
    cpf0 = cp[0];
  }
  PIN();
  const unsigned t_start = now32();
  asm volatile("" :: "s"(p.x), "s"(p.kc), "s"(p.vc), "s"(p.wq), "s"(p.wo), "s"(p.wg), "s"(p.wu), "s"(p.wd), "s"(p.out), "s"(p.ws));
  if (wave == 0 && lane < 52) flags[lane] = lane < 16 ? 0u : EMPTY;
  WAIT_LGKM0();
  __builtin_amdgcn_s_barrier();
  PIN();

  if (wave == 10) {
    const unsigned E = __hip_atomic_load((const GAS unsigned*)(p.ws + WS_EPOCH), __ATOMIC_RELAXED, __HIP_MEMORY_SCOPE_AGENT);
    const rsrc_t rx = mk_rsrc(p.x, D * 2);
    __builtin_amdgcn_raw_ptr_buffer_load_lds(rx, (LAS void*)(L + L_X), 16, lane * 16, 0, 0, 0);
    __builtin_amdgcn_raw_ptr_buffer_load_lds(rx, (LAS void*)(L + L_X + 1024), 16, 1024 + lane * 16, 0, 0, 0);
    const rsrc_t rq = mk_rsrc(p.wq, D * NQ * 2);
    const unsigned qoff = (unsigned)(((144 * (sp >> 2) + 18 * g8) * NQ + kvh * 256 + 64 * (sp & 3) + 8 * c8) * 2);
#pragma unroll
    for (int t = 0; t < 18; ++t) __builtin_amdgcn_raw_ptr_buffer_load_lds(rq, (LAS void*)(L + L_WQ + 1024 * t), 16, qoff + t * (NQ * 2), 0, 0, AUX_NT);
    PIN();
    __builtin_amdgcn_raw_buffer_store_b16((unsigned short)0, mk_rsrc(p.out, D * 2),
                                          (lg < 18 && lane < 4) ? (64 * (rfin >> 4) + 32 * ((rfin >> 3) & 1) + 8 * lane + (rfin & 7)) * 2 : (int)OOB, 0, 0);
    PIN();
    __builtin_amdgcn_s_waitcnt(0x4F73);
    const unsigned T = UNI(E) + 1u;
    asm volatile("ds_write_b32 %0, %1 offset:%2\n\ts_waitcnt lgkmcnt(0)\n\tds_write_b32 %0, %3 offset:%4" :: "v"(0u), "v"(T), "i"(L_FLAG + 4 * W_TAG), "v"(1u), "i"(L_FLAG + 4 * F_X) : "memory");
    st_sc1(rws, (idx == 0 && lane == 0) ? (unsigned)(WS_TB + xg * 128) : OOB, pack2x(t_start, 0u, mk_tag(T)));
    __builtin_amdgcn_s_waitcnt(0x0F72);
    asm volatile("ds_write_b32 %0, %1 offset:%2" :: "v"(0u), "v"(1u), "i"(L_FLAG + 4 * F_WQ) : "memory");
  }
  unsigned wave2 = wave;
  asm volatile("" : "+s"(wave2));

  if (wave2 - 1u >= 9u) {
  if (wave2 == 0) {
    const int st = sp & 3, rs = sp >> 2;
    const Sel sel = mk_sel(lane);
    const unsigned qpoff = (lane < 32 && (lane & 15) < 8) ? (unsigned)(WS_QP + ((kvh * 4 + rs) * 4 + st) * 256 + ((lane >> 4) * 8 + c8) * 16) : OOB;
    lds_wait(&flags[F_X], 1u);
    const LAS unsigned* xl = (const LAS unsigned*)(L + L_X);
    const LAS u16* xh = (const LAS u16*)(L + L_X);
    unsigned xw[5];
#pragma unroll
    for (int i = 0; i < 4; ++i) xw[i] = xl[lane + 64 * i];
    xw[4] = lane < 32 ? xl[256 + lane] : 0u;
    const unsigned xq0 = xh[144 * rs + lane], xq1 = xh[144 * rs + 64 + lane], xq2 = xh[144 * rs + 128 + (lane & 15)];
    const unsigned xg0 = xh[36 * sp + (lane < 36 ? lane : 0)];
    const unsigned xr0 = xh[64 * (rfin >> 4) + 32 * ((rfin >> 3) & 1) + 8 * (lane & 3) + (rfin & 7)];
    const Tag tag = mk_tag(flags[W_TAG]);
    float xv[10];
#pragma unroll
    for (int i = 0; i < 5; ++i) { xv[2 * i] = asf(xw[i] << 16); xv[2 * i + 1] = asf(xw[i] & 0xffff0000u); }
    const float s = ((xv[0] + xv[1]) + (xv[2] + xv[3])) + ((xv[4] + xv[5]) + (xv[6] + xv[7])) + (xv[8] + xv[9]);
    const float mean = wave_sum_s(s) * (1.f / D);
    float q2 = 0.f, q3 = 0.f;
#pragma unroll
    for (int i = 0; i < 8; i += 2) { const float d0 = xv[i] - mean, d1 = xv[i + 1] - mean; q2 = fmaf(d0, d0, q2); q3 = fmaf(d1, d1, q3); }
    { const float d0 = lane < 32 ? xv[8] - mean : 0.f, d1 = lane < 32 ? xv[9] - mean : 0.f; q2 = fmaf(d0, d0, q2); q3 = fmaf(d1, d1, q3); }
    const float var = wave_sum_s(q2 + q3) * (1.f / D);
    const float mb = rbf(mean);
    const float den = rbf(__builtin_amdgcn_sqrtf(rbf(rbf(var) + 1e-5f)));
    const float rden = __builtin_amdgcn_rcpf(den);
    auto norm = [&](unsigned raw) -> unsigned {
      const float a = rbf(asf(raw << 16) - mb);
      float qv = a * rden; qv = fmaf(fmaf(-qv, den, a), rden, qv);
      return cvtpk(qv, qv);
    };
    LAS unsigned* xq = (LAS unsigned*)(L + L_XQ);
    xq[lane] = norm(xq0); xq[64 + lane] = norm(xq1);
    { const unsigned v2 = norm(xq2); if (lane < 16) xq[128 + lane] = v2; }
    WAIT_LGKM0();
    unsigned xv18[18];
    {
      const LAS unsigned* src = xq + 18 * g8;
#pragma unroll
      for (int i = 0; i < 9; ++i) { const v2u t2 = *(const LAS v2u*)(src + 2 * i); xv18[2 * i] = t2.x; xv18[2 * i + 1] = t2.y; }
    }
    f32x4 acc = {0.f, 0.f, 0.f, 0.f};
    lds_wait(&flags[F_WQ], 1u);
#pragma unroll
    for (int t = 0; t < 18; ++t) acc = mfma(((const LAS v4u*)(L + L_WQ))[t * 64 + lane], sel_b(xv18[t], sel), acc);
    {
      const f32x4 y = strip_fold(acc);
      st_sc1(rws, qpoff, pack4(y, tag));
    }
    { const unsigned vg = norm(xg0); if (lane < 40) ((LAS unsigned*)(L + L_XG))[lane] = lane < 36 ? vg : 0u; }
    if (lane < 4) ((LAS float*)(L + L_XR))[lane] = asf(xr0 << 16);
    WAIT_LGKM0();
    if (lane == 0) flags[F_XN] = 1u;
    {
      unsigned off[4]; bool live[4]; v4u g[4];
#pragma unroll
      for (int r = 0; r < 4; ++r) { off[r] = (unsigned)(WS_QP + (kvh * 4 + r) * 1024 + lane * 16); live[r] = true; }
      __builtin_amdgcn_s_sleep(DLY_Q);
      wait_until(t_start + T_Q - UNI(flags[W_SHIFT]));
      poll<4>(rws, off, live, tag, g);
      const f32x4 q4 = (asf4(g[0]) + asf4(g[1])) + (asf4(g[2]) + asf4(g[3]));
      LAS u16* qb = (LAS u16*)(L + L_QB) + 64 * (lane >> 4) + 32 * (g8 & 1) + c8;
      const unsigned p01 = cvtpk(q4[0], q4[1]), p23 = cvtpk(q4[2], q4[3]);
      qb[0] = (u16)p01; qb[8] = (u16)(p01 >> 16); qb[16] = (u16)p23; qb[24] = (u16)(p23 >> 16);
      WAIT_LGKM0();
      if (lane == 0) flags[F_Q] = 1u;
    }
    unsigned ooff[2];
    wo_offsets(c, 8, g8, c8, ooff);
    const unsigned poff8 = strip_off(lane, 8);
    const rsrc_t ro = mk_rsrc(p.wo, NQ * D * 2);
    lds_wait_slow(&flags[F_OP], 1u);
    v4u WO[2];
    WO[0] = ld_nt(ro, ooff[0]); WO[1] = ld_nt(ro, ooff[1]);
    PIN();
    {
      const unsigned pb = (unsigned)(WS_OP + (kvh * 16 + (lane & 15)) * 1152);
      unsigned off[2] = {pb + (4 * sp + (lane >> 4)) * 16, pb + (64 + (sp >> 3)) * 16};
      bool live[2] = {true, true}; v4u g[2];
      __builtin_amdgcn_s_sleep(DLY_O);
      poll<2>(rws, off, live, tag, g);
      const float m = asf(g[1].x | (g[1].y >> 10)), l = asf(g[1].z | (g[1].w >> 10));
      const float M = row_max16(m);
      const float wgt = __builtin_amdgcn_exp2f(m - M);
      const float Ls = row_sum16(wgt * l);
      const float o = tr_sum16(wgt * asf4(g[0]), lane) * __builtin_amdgcn_rcpf(Ls);
      if ((lane & 3) == 0) ((LAS unsigned*)(L + L_OS))[lane >> 2] = cvtpk(o, o);
      WAIT_LGKM0();
    }
    wo_partial(c, poff8, *(const LAS v2u*)(L + L_OS + 8 * g8), WO, sel, tag);
    if (lg < 18) {
      lds_wait_slow(&flags[C_SP], 9u);
      __builtin_amdgcn_s_sleep(DLY_S);
      const float sv = final_sum(rws, (unsigned)(WS_SP + rfin * 16), lane, tag);
      if (lane < 16 && (lane & 3) == 0) ((LAS float*)(L + L_FIN))[lane >> 2] = sv;
      WAIT_LGKM0();
      if (lane == 0) flags[F_S] = 1u;
    }
  } else {
    const Tag tag = mk_tag(flags[W_TAG]);
    int late;
    {
      wait_until(t_start + TB_READ);
      const v4u g = untag(ld_sc1(rws, lane < 8 ? (unsigned)(WS_TB + lane * 128) : OOB), tag);
      const bool ok = lane < 8 && (resid(g) & TMASK) == 0u;
      late = ok ? (int)(t_start - (g.x | (g.y >> 10))) : 0;
      late = late > TB_MAX ? 0 : late;
      late = max(late, __builtin_amdgcn_mov_dpp(late, XOR1, 0xf, 0xf, true));
      late = max(late, __builtin_amdgcn_mov_dpp(late, XOR2, 0xf, 0xf, true));
      late = max(late, __builtin_amdgcn_mov_dpp(late, XOR7, 0xf, 0xf, true));
      late = max(UNI(late), 0);
      if (lane == 0 && late > 0) flags[W_SHIFT] = (unsigned)late;
    }
    asm volatile("" :: "v"(cpf0), "v"(cpf1), "v"(cpf2));
    const Sel sel = mk_sel(lane);
    const int g0 = 9 * (idx >> 1) + 4 * (idx & 1), odd = idx & 1;
    unsigned doff[3];
    wd_offsets(c, 8, g8, c8, doff);
    const unsigned poff8 = strip_off(lane, 8);
    const rsrc_t rd = mk_rsrc(p.wd, F * D * 2);
    const bool lc = odd && lane < 32;
    const unsigned pb = (unsigned)(WS_GP + (xg * 32 + (lane & 15)) * 2304);
    unsigned off[3] = {pb + (g0 + (lane >> 4)) * 16, pb + 16 * 2304 + (g0 + (lane >> 4)) * 16, lc ? pb + (lane >> 4) * (16 * 2304) + (g0 + 4) * 16 : OOB};
    bool live[3] = {true, true, lc}; v4u g[3];
    const int vi = (lane >> 2) & 3;
    const int col = 64 * (rfin >> 4) + 32 * ((rfin >> 3) & 1) + 8 * vi + (rfin & 7);
    wait_until(t_start + T_LAST - (unsigned)late);
    v4u WD[3];
#pragma unroll
    for (int t = 0; t < 3; ++t) WD[t] = ld_nt(rd, doff[t]);
    PIN();
    lds_wait(&flags[C_GU], 9u);
    __builtin_amdgcn_s_sleep(DLY_H);
    poll<3>(rws, off, live, tag, g);
    if (!lc) g[2] = v4u{0u, 0u, 0u, 0u};
    {
      const float gs = tr_sum16(asf4(g[0]), lane), us = tr_sum16(asf4(g[1]), lane), cs = tr_sum16(asf4(g[2]), lane);
      auto sw = __builtin_amdgcn_permlane16_swap(fbits(cs), fbits(cs), false, false);
      const bool ex = (lane & 3) == 1;
      const float gin = rbf(ex ? asf(sw[0]) : gs), uin = rbf(ex ? asf(sw[1]) : us);
      const float hv = rbf(silu_f(gin)) * uin;
      LAS unsigned* hs = (LAS unsigned*)(L + L_HS);
      if ((lane & 3) == 0 || (ex && lane < 16)) hs[ex ? 16 + vi : lane >> 2] = cvtpk(hv, hv);
    }
    WAIT_LGKM0();
    wd_partial(c, poff8, *(const LAS v2u*)(L + L_HS + 8 * g8), ((const LAS unsigned*)(L + L_HS))[16 + (g8 & 3)], WD, sel, tag);
    if (lg < 18) {
      const float xr = ((const LAS float*)(L + L_XR))[vi];
      lds_wait(&flags[C_FP], 9u);
      __builtin_amdgcn_s_sleep(DLY_F);
      const float fv = final_sum(rws, (unsigned)(WS_FP + rfin * 16), lane, tag);
      const float gate = rbf(silu_f(rbf(fv)));
      lds_wait(&flags[F_S], 1u);
      const float sv = ((const LAS float*)(L + L_FIN))[vi];
      const float res = xr + rbf(rbf(sv) * gate);
      if (lane < 16 && (lane & 3) == 0) p.out[col] = (u16)cvtpk(res, res);
      if (b == 0 && lane == 0) __hip_atomic_store((GAS unsigned*)(p.ws + WS_EPOCH), flags[W_TAG], __ATOMIC_RELAXED, __HIP_MEMORY_SCOPE_AGENT);
    }
  }
  } else {
  const int st = wave - 1;
  const Sel sel = mk_sel(lane);
  const rsrc_t rg = mk_rsrc((idx >> 4) ? p.wu : p.wg, D * F * 2);
  unsigned goff, goff4, ooff[2], doff[3];
  auto late_addr = [&]() {
    PIN();
    int ln = lane; asm volatile("" : "+v"(ln));
    const int g8_ = ln >> 3, c8_ = ln & 7;
    const unsigned gcol = (unsigned)(576 * xg + 64 * st + 8 * c8_);
    goff = (unsigned)(((36 * sp + 4 * g8_) * F + gcol) * 2);
    goff4 = g8_ < 4 ? (unsigned)(((36 * sp + 32 + g8_) * F + gcol) * 2) : OOB;
    wo_offsets(c, st, g8_, c8_, ooff);
    wd_offsets(c, st, g8_, c8_, doff);
    PIN();
  };
  const unsigned poff = strip_off(lane, st);
  v4u G[5];
  unsigned tn;
#define SLOT(dt) do { wait_until(tn); tn += (dt); } while (0)
  auto issue_g = [&](int t) { SLOT(DT2); G[t] = ld_nt(rg, t < 4 ? goff + t * (F * 2) : goff4); PIN(); };
  v4u gb[5];
  auto gu_frags = [&]() {
    const v4u x4 = *(const LAS v4u*)(L + L_XG + 16 * g8);
    const unsigned x5 = ((const LAS unsigned*)(L + L_XG))[32 + g8];
#pragma unroll
    for (int t = 0; t < 4; ++t) gb[t] = sel_b(x4[t], sel);
    gb[4] = sel_b(x5, sel);
    PIN();
  };
  auto do_gu = [&](const Tag& tag) {
    f32x4 acc = {0.f, 0.f, 0.f, 0.f};
#pragma unroll
    for (int t = 0; t < 5; ++t) acc = mfma(G[t], gb[t], acc);
    publish_at(c, poff + WS_GP + (xg * 32 + idx) * 2304, acc, tag);
    if (lane == 0) lds_add(&flags[C_GU]);
    PIN();
  };

  if (wave > 8) {
    tn = now32() + TSTART + 2 * DT1;
    late_addr();
#pragma unroll
    for (int t = 0; t < 5; ++t) { issue_g(t); tn += W9GAP - DT2; }
    lds_wait_slow(&flags[F_XN], 1u);
    const Tag tag = mk_tag(flags[W_TAG]);
    gu_frags();
    do_gu(tag);
    lds_wait(&flags[C_ATT], 8u);
    const int hh = g8 & 1;
    f32x2 ml[8]; f32x4 po[8];
#pragma unroll
    for (int w = 0; w < 8; ++w) { ml[w] = ((const LAS f32x2*)(L + L_ML))[w * 2 + hh]; po[w] = ((const LAS f32x4*)(L + L_PO))[w * 64 + lane]; }
    const float M = fmaxf(fmaxf(fmaxf(ml[0].x, ml[1].x), fmaxf(ml[2].x, ml[3].x)), fmaxf(fmaxf(ml[4].x, ml[5].x), fmaxf(ml[6].x, ml[7].x)));
    f32x4 o = {0.f, 0.f, 0.f, 0.f}; float Ls = 0.f;
#pragma unroll
    for (int w = 0; w < 8; ++w) {
      const float sc = __builtin_amdgcn_exp2f(ml[w].x - M);
      Ls = fmaf(sc, ml[w].y, Ls);
      o += sc * po[w];
    }
    const unsigned pbase = (unsigned)(WS_OP + (kvh * 16 + sp) * 1152);
    st_sc1(rws, pbase + (hh * 32 + (lane >> 4) * 8 + c8) * 16, pack4(o, tag));
    if ((lane & 0x37) == 0) st_sc1(rws, pbase + (64 + hh) * 16, pack2x(fbits(M), fbits(Ls), tag));
    if (lane == 0) flags[F_OP] = 1u;
    PIN();
  } else {
    const size_t tile = ((size_t)kvh * SK + (SK - WIN)) * DH;
    const rsrc_t rk = mk_rsrc(p.kc + tile, WIN * DH * 2);
    const rsrc_t rv = mk_rsrc(p.vc + tile, WIN * DH * 2);
    const int cbase = (st * 8 * 16 + sp) * 1024;
    const unsigned koff = (unsigned)(((lane >> 2) & 3) * 256 + ((lane & 3) + 4 * (lane >> 4)) * 16);
    tn = now32() + TSTART + (st * DT1) / 8;
    v4u K[8], V[8];
#define LDK(t) do { SLOT(DT1); if (t >= KWIN) WAIT_VM(KWIN - 1); K[t] = __builtin_amdgcn_raw_buffer_load_b128(rk, (int)koff, cbase + t * 16384, AUX_NT); PIN(); } while (0)
#define LDV(t) do { SLOT(DT1); WAIT_VM(KWIN - 1); V[t] = __builtin_amdgcn_raw_buffer_load_b128(rv, lane * 16, cbase + t * 16384, AUX_NT); PIN(); } while (0)
    unsigned shifted = 0;
#define CATCH_UP() do { const unsigned sh_ = UNI(flags[W_SHIFT]); tn -= sh_ - shifted; shifted = sh_; } while (0)
#pragma unroll
    for (int t = 0; t < 8; ++t) { LDK(t); if (t == 1) late_addr(); if (t == 5) CATCH_UP(); }
    constexpr float C2 = 0.08838834764831845f * 1.4426950408889634f;
    unsigned pk[8];
    auto qk_softmax = [&]() {
      const v4u qf = *(const LAS v4u*)(L + L_QB + ((g8 & 1) * 128 + ((lane & 3) + 4 * (lane >> 4)) * 8) * 2);
      float s[8];
#pragma unroll
      for (int t = 0; t < 8; ++t) {
        const f32x4 d = mfma(K[t], qf, f32x4{0.f, 0.f, 0.f, 0.f});
        s[t] = (dpp<0x00>(d[0]) + dpp<0x55>(d[1])) + (dpp<0xAA>(d[2]) + dpp<0xFF>(d[3]));
      }
      float m = fmaxf(fmaxf(fmaxf(s[0], s[1]), fmaxf(s[2], s[3])), fmaxf(fmaxf(s[4], s[5]), fmaxf(s[6], s[7])));
      m = x16_max(m) * C2;
      float l = 0.f;
#pragma unroll
      for (int t = 0; t < 8; ++t) { const float pr = __builtin_amdgcn_exp2f(fmaf(s[t], C2, -m)); l += pr; pk[t] = cvtpk(pr, pr); }
      l = x16_sum(l);
      if ((lane & 0x37) == 0) ((LAS f32x2*)(L + L_ML))[st * 2 + g8] = f32x2{m, l};
      PIN();
    };
    auto pv = [&]() {
      f32x4 oacc = {0.f, 0.f, 0.f, 0.f};
#pragma unroll
      for (int t = 0; t < 8; ++t) oacc = mfma(V[t], sel_b(pk[t], sel), oacc);
      ((LAS f32x4*)(L + L_PO))[st * 64 + lane] = oacc;
      WAIT_LGKM0();
      if (lane == 0) lds_add(&flags[C_ATT]);
      PIN();
    };
#pragma unroll
    for (int t = 0; t < 8; ++t) { LDV(t); if (t == 0 || t == 3) CATCH_UP(); }
    tn -= (st * DT1) / 8;
    bool early = UNI(flags[F_Q]) != 0u;
    if (early) qk_softmax();
    issue_g(0); issue_g(1);
    if (!early && UNI(flags[F_Q]) != 0u) { qk_softmax(); early = true; }
    if (early) pv();
    issue_g(2); issue_g(3); issue_g(4);
    if (!early) lds_wait(&flags[F_XN], 1u);
    const Tag tag = mk_tag(flags[W_TAG]);
    gu_frags();
    const rsrc_t ro = mk_rsrc(p.wo, NQ * D * 2);
    const rsrc_t rd = mk_rsrc(p.wd, F * D * 2);
    v4u WO[2], WD[3];
    f32x4 gacc = {0.f, 0.f, 0.f, 0.f};
#pragma unroll
    for (int t = 0; t < 3; ++t) gacc = mfma(G[t], gb[t], gacc);
    PIN();
    tn += DTD - DT2;
    SLOT(DTD); WD[0] = ld_nt(rd, doff[0]); PIN();
    gacc = mfma(G[3], gb[3], gacc);
    gacc = mfma(G[4], gb[4], gacc);
    publish_at(c, poff + WS_GP + (xg * 32 + idx) * 2304, gacc, tag);
    if (lane == 0) lds_add(&flags[C_GU]);
    PIN();
    if (!early) { lds_wait(&flags[F_Q], 1u); qk_softmax(); pv(); }
    SLOT(DTD); WD[1] = ld_nt(rd, doff[1]); PIN();
    if (idx & 1) SLOT(DTD);
    WD[2] = ld_nt(rd, doff[2]); PIN();
    tn += DTO - DTD;
    SLOT(DT2); WO[0] = ld_nt(ro, ooff[0]); PIN();
    SLOT(DT2); WO[1] = ld_nt(ro, ooff[1]); PIN();
    v2u h2, o2; unsigned h3, spins = 0; bool hh;
#pragma nounroll
    for (;;) {
      h2 = *(const volatile LAS v2u*)(L + L_HS + 8 * g8); h3 = ((const volatile LAS unsigned*)(L + L_HS))[16 + (g8 & 3)];
      o2 = *(const volatile LAS v2u*)(L + L_OS + 8 * g8);
      hh = __all(h2.x != EMPTY && h2.y != EMPTY && h3 != EMPTY);
      if (hh || __all(o2.x != EMPTY && o2.y != EMPTY) || ++spins > (SPIN_CAP << 6)) break;
    }
    if (hh) {
      wd_partial(c, poff, h2, h3, WD, sel, tag);
#pragma nounroll
      while (!__all(o2.x != EMPTY && o2.y != EMPTY) && ++spins <= (SPIN_CAP << 6)) o2 = *(const volatile LAS v2u*)(L + L_OS + 8 * g8);
      wo_partial(c, poff, o2, WO, sel, tag);
    } else {
      wo_partial(c, poff, o2, WO, sel, tag);
#pragma nounroll
      while (!__all(h2.x != EMPTY && h2.y != EMPTY && h3 != EMPTY) && ++spins <= (SPIN_CAP << 6)) {
        h2 = *(const volatile LAS v2u*)(L + L_HS + 8 * g8); h3 = ((const volatile LAS unsigned*)(L + L_HS))[16 + (g8 & 3)];
      }
      wd_partial(c, poff, h2, h3, WD, sel, tag);
    }
  }
  }
}

}

extern "C" void kernel_launch(void* const* d_in, const int* in_sizes, int n_in, void* d_out, int out_size, void* d_ws, size_t ws_size, hipStream_t stream) {
  Params p;
  p.x = (const u16*)d_in[0]; p.kc = (const u16*)d_in[1]; p.vc = (const u16*)d_in[2];
  p.wq = (const u16*)d_in[3]; p.wo = (const u16*)d_in[4];
  p.wg = (const u16*)d_in[6]; p.wu = (const u16*)d_in[7]; p.wd = (const u16*)d_in[8];
  p.out = (u16*)d_out; p.ws = (unsigned char*)d_ws;
  (void)hipFuncSetAttribute((const void*)fused_decode, hipFuncAttributeMaxDynamicSharedMemorySize, LDS_BYTES);
  hipLaunchKernelGGL(fused_decode, dim3(NWG), dim3(NTHR), LDS_BYTES, stream, p);
}
```

```cpp
#include <hip/hip_runtime.h>
#include <hip/hip_bf16.h>
#include <stdint.h>

#define GAS __attribute__((address_space(1)))
#define LAS __attribute__((address_space(3)))
typedef unsigned v4u __attribute__((ext_vector_type(4)));
typedef unsigned v2u __attribute__((ext_vector_type(2)));
typedef float f32x4 __attribute__((ext_vector_type(4)));
typedef float f32x2 __attribute__((ext_vector_type(2)));
typedef short bf16x8 __attribute__((ext_vector_type(8)));
typedef __bf16 bf16x2 __attribute__((ext_vector_type(2)));
typedef unsigned short u16;
typedef __amdgpu_buffer_rsrc_t rsrc_t;

#define KWIN 4
#define DT1 33
#define DT2 33
#define DTD 24
#define DTO 45
#define DRY 1
#define TSTART 10
#define W9GAP 70
#define T_Q 450
#define T_LAST 770
#define CODE_BYTES 19200
#define CODE_PF_WGS 2
#define TB_READ 170
#define TB_MAX 300
#define DLY_Q 24
#define DLY_O 30
#define DLY_H 22
#define DLY_F 22
#define DLY_S 28
#define RETRY 8

namespace {

constexpr int D = 576, SK = 65536, DH = 128, WIN = 4096, F = 4608, NQ = 4096;
constexpr int NWG = 256, NWAVE = 11, NTHR = NWAVE * 64;
constexpr int LDS_BYTES = 84 * 1024;

constexpr int WS_EPOCH = 0;
constexpr int WS_TB = 256;
constexpr int WS_QP = WS_TB + 1024;
constexpr int WS_OP = WS_QP + 16 * 4096;
constexpr int WS_GP = WS_OP + 16 * 16 * 1152;
constexpr int WS_SP = WS_GP + 8 * 32 * 2304;
constexpr int WS_FP = WS_SP + 256 * 2304;
constexpr int WS_END = WS_FP + 256 * 2304;

constexpr int L_FLAG = 0;
constexpr int L_OS = 64;
constexpr int L_HS = L_OS + 64;
constexpr int L_XQ = L_HS + 128;
constexpr int L_XG = L_XQ + 576;
constexpr int L_XR = L_XG + 160;
constexpr int L_QB = L_XR + 16;
constexpr int L_ML = L_QB + 512;
constexpr int L_FIN = L_ML + 128;
constexpr int L_PO = L_FIN + 16;
constexpr int L_X = L_PO + 8192;
constexpr int L_WQ = L_X + 2048;
constexpr int L_DUMMY = L_WQ + 18 * 1024;
constexpr int L_TS = L_DUMMY + 128;
static_assert(L_PO % 16 == 0 && L_XQ % 16 == 0 && L_XG % 16 == 0 && L_QB % 16 == 0 && L_OS % 16 == 0 && L_HS % 16 == 0 && L_XR % 16 == 0 && L_X % 16 == 0, "align");
enum { F_XN = 0, F_Q = 1, C_ATT = 2, C_GU = 3, F_OP = 4, C_SP = 5, C_FP = 6, F_S = 7, W_TAG = 8, W_SHIFT = 9, F_X = 10, F_WQ = 11 };

constexpr int AUX_NT = 2, AUX_SC1 = 16;
constexpr unsigned VMASK = 0xFFFFFC00u, TMASK = 0x3FFu, OOB = 0xFFFFFF00u;
constexpr unsigned EMPTY = 0xFFFF0000u;
constexpr unsigned SPIN_CAP = 1u << 17;

struct Params {
  const u16* x; const u16* kc; const u16* vc; const u16* wq; const u16* wo;
  const u16* wg; const u16* wu; const u16* wd; u16* out; unsigned char* ws;
};

__device__ __forceinline__ unsigned fbits(float f) { return __builtin_bit_cast(unsigned, f); }
__device__ __forceinline__ float asf(unsigned u) { return __builtin_bit_cast(float, u); }
__device__ __forceinline__ unsigned cvtpk(float a, float b) { bf16x2 r = {(__bf16)a, (__bf16)b}; return __builtin_bit_cast(unsigned, r); }
__device__ __forceinline__ float rbf(float f) { return asf(cvtpk(0.f, f) & 0xffff0000u); }
__device__ __forceinline__ float silu_f(float x) { return x * __builtin_amdgcn_rcpf(1.f + __builtin_amdgcn_exp2f(x * -1.4426950408889634f)); }
__device__ __forceinline__ f32x4 mfma(v4u a, v4u b, f32x4 c) {
  return __builtin_amdgcn_mfma_f32_16x16x32_bf16(__builtin_bit_cast(bf16x8, a), __builtin_bit_cast(bf16x8, b), c, 0, 0, 0);
}
template <int CTRL> __device__ __forceinline__ float dpp(float x) {
  return asf(__builtin_amdgcn_mov_dpp(__builtin_bit_cast(int, x), CTRL, 0xf, 0xf, true));
}
template <int CTRL, int RM> __device__ __forceinline__ float dppm(float x) {
  return asf(__builtin_amdgcn_update_dpp(0, __builtin_bit_cast(int, x), CTRL, RM, 0xf, true));
}
constexpr int XOR1 = 0xB1, XOR2 = 0x4E, XOR7 = 0x141, ROR8 = 0x128;
__device__ __forceinline__ float x16_max(float x) {
  auto s = __builtin_amdgcn_permlane16_swap(fbits(x), fbits(x), false, false);
  x = fmaxf(asf(s[0]), asf(s[1]));
  auto t = __builtin_amdgcn_permlane32_swap(fbits(x), fbits(x), false, false);
  return fmaxf(asf(t[0]), asf(t[1]));
}
__device__ __forceinline__ float x16_sum(float x) {
  auto s = __builtin_amdgcn_permlane16_swap(fbits(x), fbits(x), false, false);
  x = asf(s[0]) + asf(s[1]);
  auto t = __builtin_amdgcn_permlane32_swap(fbits(x), fbits(x), false, false);
  return asf(t[0]) + asf(t[1]);
}
__device__ __forceinline__ float row_sum16(float x) { x += dpp<XOR1>(x); x += dpp<XOR2>(x); x += dpp<XOR7>(x); x += dpp<ROR8>(x); return x; }
__device__ __forceinline__ float row_max16(float x) { x = fmaxf(x, dpp<XOR1>(x)); x = fmaxf(x, dpp<XOR2>(x)); x = fmaxf(x, dpp<XOR7>(x)); x = fmaxf(x, dpp<ROR8>(x)); return x; }
__device__ __forceinline__ float wave_sum_s(float s) {
  s += dpp<0x111>(s); s += dpp<0x112>(s); s += dpp<0x114>(s); s += dpp<0x118>(s);
  s += dppm<0x142, 0xa>(s); s += dppm<0x143, 0xc>(s);
  return asf(__builtin_amdgcn_readlane(fbits(s), 63));
}
__device__ __forceinline__ float tr_sum16(f32x4 v, int lane) {
  const bool b3 = lane & 8, b2 = lane & 4;
  const float a = (b3 ? v[2] : v[0]) + dpp<ROR8>(b3 ? v[0] : v[2]);
  const float b = (b3 ? v[3] : v[1]) + dpp<ROR8>(b3 ? v[1] : v[3]);
  float r = (b2 ? b : a) + dpp<XOR7>(b2 ? a : b);
  r += dpp<XOR2>(r); r += dpp<XOR1>(r);
  return r;
}

__device__ __forceinline__ rsrc_t mk_rsrc(const void* p, unsigned bytes) {
  return __builtin_amdgcn_make_buffer_rsrc((void*)p, 0, (int)bytes, 0x00020000);
}
__device__ __forceinline__ v4u ld_nt(rsrc_t r, unsigned off) { return __builtin_amdgcn_raw_buffer_load_b128(r, (int)off, 0, AUX_NT); }
__device__ __forceinline__ v4u ld_sc1(rsrc_t r, unsigned off) { return __builtin_amdgcn_raw_buffer_load_b128(r, (int)off, 0, AUX_SC1); }
__device__ __forceinline__ void st_sc1(rsrc_t r, unsigned off, v4u g) { __builtin_amdgcn_raw_buffer_store_b128(g, r, (int)off, 0, AUX_SC1); }
__device__ __forceinline__ unsigned now32() { unsigned long long t; asm volatile("s_memrealtime %0\n\ts_waitcnt lgkmcnt(0)" : "=s"(t) :: "memory"); return (unsigned)t; }
__device__ __forceinline__ void wait_until(unsigned t) {
#pragma nounroll
  while ((int)(now32() - t) < 0) __builtin_amdgcn_s_sleep(1);
}
#define PIN() __builtin_amdgcn_sched_barrier(0)
#define CBAR() asm volatile("" ::: "memory")
#define WAIT_VM(n) asm volatile("s_waitcnt vmcnt(%0)" ::"i"(n) : "memory")
#define WAIT_LGKM0() asm volatile("s_waitcnt lgkmcnt(0)" ::: "memory")
#define UNI(x) __builtin_amdgcn_readfirstlane(x)

struct Tag { unsigned t0, t1, t2, t3; };
__device__ __forceinline__ Tag mk_tag(unsigned T) { return Tag{T & TMASK, (T + 0x155u) & TMASK, (T + 0x2AAu) & TMASK, (T + 0x0FFu) & TMASK}; }
__device__ __forceinline__ v4u pack4(f32x4 v, const Tag& t) {
  return v4u{(fbits(v.x) & VMASK) | t.t0, (fbits(v.y) & VMASK) | t.t1, (fbits(v.z) & VMASK) | t.t2, (fbits(v.w) & VMASK) | t.t3};
}
__device__ __forceinline__ v4u pack2x(unsigned a, unsigned b, const Tag& t) {
  return v4u{(a & VMASK) | t.t0, ((a & TMASK) << 10) | t.t1, (b & VMASK) | t.t2, ((b & TMASK) << 10) | t.t3};
}
__device__ __forceinline__ v4u untag(v4u g, const Tag& t) { return v4u{g.x ^ t.t0, g.y ^ t.t1, g.z ^ t.t2, g.w ^ t.t3}; }
__device__ __forceinline__ unsigned resid(v4u u) { return (u.x | u.y | u.z) | u.w; }
__device__ __forceinline__ f32x4 asf4(v4u u) { return __builtin_bit_cast(f32x4, u); }

template <int N>
__device__ __forceinline__ void poll(rsrc_t r, const unsigned (&off)[N], const bool (&live)[N], const Tag& tag, v4u (&res)[N], bool dry = false) {
  unsigned spins = 0;
#pragma nounroll
  for (;;) {
    v4u a[N];
#pragma unroll
    for (int i = 0; i < N; ++i) a[i] = ld_sc1(r, dry ? OOB : off[i]);
    CBAR();
    unsigned bad = 0;
#pragma unroll
    for (int i = 0; i < N; ++i) { res[i] = untag(a[i], tag); bad |= live[i] ? resid(res[i]) : 0u; }
    if (dry || __all((bad & TMASK) == 0u) || ++spins > SPIN_CAP) break;
    __builtin_amdgcn_s_sleep(RETRY);
  }
}
__device__ __forceinline__ float final_sum(rsrc_t rws, unsigned base, int lane, const Tag& tag, bool dry) {
  unsigned off[4]; bool live[4]; v4u g[4];
#pragma unroll
  for (int i = 0; i < 4; ++i) { off[i] = base + (unsigned)((i * 64 + lane) * 2304); live[i] = true; }
  poll<4>(rws, off, live, tag, g, dry);
  const f32x4 v = (asf4(g[0]) + asf4(g[1])) + (asf4(g[2]) + asf4(g[3]));
  return x16_sum(tr_sum16(v, lane));
}

__device__ __forceinline__ void lds_wait(volatile LAS unsigned* f, unsigned want) {
  unsigned spins = 0;
#pragma nounroll
  while (*f < want) { if (++spins > (SPIN_CAP << 6)) break; }
  CBAR();
}
__device__ __forceinline__ void lds_wait_slow(volatile LAS unsigned* f, unsigned want) {
  unsigned spins = 0;
#pragma nounroll
  while (*f < want) { __builtin_amdgcn_s_sleep(2); if (++spins > (SPIN_CAP << 4)) break; }
  CBAR();
}
__device__ __forceinline__ void lds_add(volatile LAS unsigned* f) { __hip_atomic_fetch_add((LAS unsigned*)f, 1u, __ATOMIC_RELAXED, __HIP_MEMORY_SCOPE_WORKGROUP); }

struct Sel { unsigned m0, m1, m2, m3; };
__device__ __forceinline__ Sel mk_sel(int lane) {
  const unsigned h = (lane & 1) ? 0xFFFF0000u : 0x0000FFFFu; const int r = (lane & 7) >> 1;
  return Sel{r == 0 ? h : 0u, r == 1 ? h : 0u, r == 2 ? h : 0u, r == 3 ? h : 0u};
}
__device__ __forceinline__ v4u sel_b(unsigned dup, const Sel& s) { return v4u{dup & s.m0, dup & s.m1, dup & s.m2, dup & s.m3}; }
__device__ __forceinline__ f32x4 strip_fold(f32x4 d) {
  f32x4 y;
#pragma unroll
  for (int i = 0; i < 4; ++i) {
    const float t = dpp<ROR8>(d[i]);
    auto s = __builtin_amdgcn_permlane32_swap(fbits(t), fbits(t), false, false);
    y[i] = d[i] + asf(s[1]);
  }
  return y;
}

struct Ctx {
  LAS unsigned char* L; volatile LAS unsigned* flags; rsrc_t rws;
  int lane, g8, c8, b, xg, idx, kvh, sp;
};
__device__ __forceinline__ void wo_offsets(const Ctx& c, int st, int g8, int c8, unsigned (&off)[2]) {
  const int hrow = (2 * c.kvh + (c.sp >> 3)) * 128 + 32 * ((c.sp >> 1) & 3) + 4 * (c.sp & 1);
#pragma unroll
  for (int t = 0; t < 2; ++t) { const int u = 2 * g8 + t; off[t] = (unsigned)(((hrow + 8 * (u & 3) + (u >> 2)) * D + 64 * st + 8 * c8) * 2); }
}
__device__ __forceinline__ void wd_offsets(const Ctx& c, int st, int g8, int c8, unsigned (&off)[3]) {
  const int odd = c.idx & 1, g0 = 9 * (c.idx >> 1) + 4 * odd;
#pragma unroll
  for (int t = 0; t < 3; ++t) {
    const int u = t < 2 ? 2 * g8 + t : 16 + (g8 & 3), Gi = g0 + (u >> 2);
    const int row = 576 * c.xg + 64 * (Gi >> 4) + 32 * ((Gi >> 3) & 1) + 8 * (u & 3) + (Gi & 7);
    off[t] = (t < 2 || (odd && g8 < 4)) ? (unsigned)((row * D + 64 * st + 8 * c8) * 2) : OOB;
  }
}
__device__ __forceinline__ unsigned strip_off(int lane, int st) {
  unsigned o = (lane < 32 && (lane & 15) < 8) ? (unsigned)((st * 16 + (lane >> 4) * 8 + (lane & 7)) * 16) : 0x80000000u;
  asm volatile("" : "+v"(o));
  return o;
}
__device__ __forceinline__ void publish_at(const Ctx& c, unsigned off, f32x4 acc, const Tag& tag) {
  st_sc1(c.rws, off, pack4(strip_fold(acc), tag));
}
__device__ __forceinline__ void wo_partial(const Ctx& c, unsigned poff, v2u o2, const v4u (&WO)[2], const Sel& sel, const Tag& tag, bool dry) {
  f32x4 acc = {0.f, 0.f, 0.f, 0.f};
  acc = mfma(WO[0], sel_b(o2.x, sel), acc);
  acc = mfma(WO[1], sel_b(o2.y, sel), acc);
  publish_at(c, (dry ? 0x80000000u : poff) + WS_SP + c.b * 2304, acc, tag);
  if (c.lane == 0 && !dry) lds_add(&c.flags[C_SP]);
  PIN();
}
__device__ __forceinline__ void wd_partial(const Ctx& c, unsigned poff, v2u h2, unsigned h3, const v4u (&WD)[3], const Sel& sel, const Tag& tag, bool dry) {
  f32x4 acc = {0.f, 0.f, 0.f, 0.f};
  acc = mfma(WD[0], sel_b(h2.x, sel), acc);
  acc = mfma(WD[1], sel_b(h2.y, sel), acc);
  acc = mfma(WD[2], sel_b(c.g8 < 4 ? h3 : 0u, sel), acc);
  publish_at(c, (dry ? 0x80000000u : poff) + WS_FP + c.b * 2304, acc, tag);
  if (c.lane == 0 && !dry) lds_add(&c.flags[C_FP]);
  PIN();
}

#define STAMP(i) do {} while (0)
#define STAMP_INIT() do {} while (0)
#define STAMP_FLUSH() do {} while (0)

__global__ void __launch_bounds__(NTHR, 1) fused_decode(Params p) {
  extern __shared__ __attribute__((aligned(16))) unsigned char smem[];
  LAS unsigned char* L = (LAS unsigned char*)smem;
  volatile LAS unsigned* flags = (volatile LAS unsigned*)(L + L_FLAG);
  const int lane = threadIdx.x & 63;
  const int wave = UNI(threadIdx.x >> 6);
  const int b = blockIdx.x, xg = b & 7, idx = b >> 3;
  const int kvh = 2 * xg + (idx >> 4), sp = idx & 15;
  const rsrc_t rws = mk_rsrc(p.ws, WS_END);
  const int lg = (idx >> 3) * 8 + xg;
  const int rfin = lg * 8 + (idx & 7);
  const int g8 = lane >> 3, c8 = lane & 7;
  const Ctx c{L, flags, rws, lane, g8, c8, b, xg, idx, kvh, sp};

  unsigned long long code_base;
  asm volatile("s_getpc_b64 %0" : "=s"(code_base));
  code_base &= ~255ull;
  unsigned cpf0 = 0, cpf1 = 0, cpf2 = 0;
  if ((threadIdx.x >> 6) == 10 && (int)(blockIdx.x >> 3) < CODE_PF_WGS) {
    const GAS unsigned* cp = (const GAS unsigned*)code_base + (threadIdx.x & 63) * 32;
    if ((threadIdx.x & 63) * 128 + 16384 < CODE_BYTES) cpf2 = cp[4096];
    if ((threadIdx.x & 63) * 128 + 8192 < CODE_BYTES) cpf1 = cp[2048];
    cpf0 = cp[0];
  }
  PIN();
  STAMP_INIT();
  STAMP(0);
  const unsigned t_start = now32();
  asm volatile("" :: "s"(p.x), "s"(p.kc), "s"(p.vc), "s"(p.wq), "s"(p.wo), "s"(p.wg), "s"(p.wu), "s"(p.wd), "s"(p.out), "s"(p.ws));
  if (wave == 0 && lane < 52) flags[lane] = lane < 16 ? 0u : EMPTY;
  WAIT_LGKM0();
  __builtin_amdgcn_s_barrier();
  PIN();
  STAMP(12);

  if (wave == 10) {
    const unsigned E = __hip_atomic_load((const GAS unsigned*)(p.ws + WS_EPOCH), __ATOMIC_RELAXED, __HIP_MEMORY_SCOPE_AGENT);
    const rsrc_t rx = mk_rsrc(p.x, D * 2);
    __builtin_amdgcn_raw_ptr_buffer_load_lds(rx, (LAS void*)(L + L_X), 16, lane * 16, 0, 0, 0);
    __builtin_amdgcn_raw_ptr_buffer_load_lds(rx, (LAS void*)(L + L_X + 1024), 16, 1024 + lane * 16, 0, 0, 0);
    const rsrc_t rq = mk_rsrc(p.wq, D * NQ * 2);
    const unsigned qoff = (unsigned)(((144 * (sp >> 2) + 18 * g8) * NQ + kvh * 256 + 64 * (sp & 3) + 8 * c8) * 2);
#pragma unroll
    for (int t = 0; t < 18; ++t) __builtin_amdgcn_raw_ptr_buffer_load_lds(rq, (LAS void*)(L + L_WQ + 1024 * t), 16, qoff + t * (NQ * 2), 0, 0, AUX_NT);
    PIN();
    __builtin_amdgcn_raw_buffer_store_b16((unsigned short)0, mk_rsrc(p.out, D * 2),
                                          (lg < 18 && lane < 4) ? (64 * (rfin >> 4) + 32 * ((rfin >> 3) & 1) + 8 * lane + (rfin & 7)) * 2 : (int)OOB, 0, 0);
    PIN();
    STAMP(13);
    __builtin_amdgcn_s_waitcnt(0x4F73);
    const unsigned T = UNI(E) + 1u;
    asm volatile("ds_write_b32 %0, %1 offset:%2\n\ts_waitcnt lgkmcnt(0)\n\tds_write_b32 %0, %3 offset:%4" :: "v"(0u), "v"(T), "i"(L_FLAG + 4 * W_TAG), "v"(1u), "i"(L_FLAG + 4 * F_X) : "memory");
    st_sc1(rws, (idx == 0 && lane == 0) ? (unsigned)(WS_TB + xg * 128) : OOB, pack2x(t_start, 0u, mk_tag(T)));
    __builtin_amdgcn_s_waitcnt(0x0F72);
    asm volatile("ds_write_b32 %0, %1 offset:%2" :: "v"(0u), "v"(1u), "i"(L_FLAG + 4 * F_WQ) : "memory");
    STAMP(14);
  }
  unsigned wave2 = wave;
  asm volatile("" : "+s"(wave2));

  if (wave2 - 1u >= 9u) {
  if (wave2 == 0) {
    const int st = sp & 3, rs = sp >> 2;
    const Sel sel = mk_sel(lane);
    const unsigned qpoff = (lane < 32 && (lane & 15) < 8) ? (unsigned)(WS_QP + ((kvh * 4 + rs) * 4 + st) * 256 + ((lane >> 4) * 8 + c8) * 16) : OOB;
    lds_wait(&flags[F_X], 1u);
    const LAS unsigned* xl = (const LAS unsigned*)(L + L_X);
    const LAS u16* xh = (const LAS u16*)(L + L_X);
    unsigned xw[5];
#pragma unroll
    for (int i = 0; i < 4; ++i) xw[i] = xl[lane + 64 * i];
    xw[4] = lane < 32 ? xl[256 + lane] : 0u;
    const unsigned xq0 = xh[144 * rs + lane], xq1 = xh[144 * rs + 64 + lane], xq2 = xh[144 * rs + 128 + (lane & 15)];
    const unsigned xg0 = xh[36 * sp + (lane < 36 ? lane : 0)];
    const unsigned xr0 = xh[64 * (rfin >> 4) + 32 * ((rfin >> 3) & 1) + 8 * (lane & 3) + (rfin & 7)];
    const Tag tag = mk_tag(flags[W_TAG]);
    float xv[10];
#pragma unroll
    for (int i = 0; i < 5; ++i) { xv[2 * i] = asf(xw[i] << 16); xv[2 * i + 1] = asf(xw[i] & 0xffff0000u); }
    const float s = ((xv[0] + xv[1]) + (xv[2] + xv[3])) + ((xv[4] + xv[5]) + (xv[6] + xv[7])) + (xv[8] + xv[9]);
    const float mean = wave_sum_s(s) * (1.f / D);
    float q2 = 0.f, q3 = 0.f;
#pragma unroll
    for (int i = 0; i < 8; i += 2) { const float d0 = xv[i] - mean, d1 = xv[i + 1] - mean; q2 = fmaf(d0, d0, q2); q3 = fmaf(d1, d1, q3); }
    { const float d0 = lane < 32 ? xv[8] - mean : 0.f, d1 = lane < 32 ? xv[9] - mean : 0.f; q2 = fmaf(d0, d0, q2); q3 = fmaf(d1, d1, q3); }
    const float var = wave_sum_s(q2 + q3) * (1.f / D);
    const float mb = rbf(mean);
    const float den = rbf(__builtin_amdgcn_sqrtf(rbf(rbf(var) + 1e-5f)));
    const float rden = __builtin_amdgcn_rcpf(den);
    auto norm = [&](unsigned raw) -> unsigned {
      const float a = rbf(asf(raw << 16) - mb);
      float qv = a * rden; qv = fmaf(fmaf(-qv, den, a), rden, qv);
      return cvtpk(qv, qv);
    };
    LAS unsigned* xq = (LAS unsigned*)(L + L_XQ);
    xq[lane] = norm(xq0); xq[64 + lane] = norm(xq1);
    { const unsigned v2 = norm(xq2); if (lane < 16) xq[128 + lane] = v2; }
    WAIT_LGKM0();
    unsigned xv18[18];
    {
      const LAS unsigned* src = xq + 18 * g8;
#pragma unroll
      for (int i = 0; i < 9; ++i) { const v2u t2 = *(const LAS v2u*)(src + 2 * i); xv18[2 * i] = t2.x; xv18[2 * i + 1] = t2.y; }
    }
    STAMP(1);
    f32x4 acc = {0.f, 0.f, 0.f, 0.f};
    lds_wait(&flags[F_WQ], 1u);
#pragma unroll
    for (int t = 0; t < 18; ++t) acc = mfma(((const LAS v4u*)(L + L_WQ))[t * 64 + lane], sel_b(xv18[t], sel), acc);
    {
      const f32x4 y = strip_fold(acc);
      st_sc1(rws, qpoff, pack4(y, tag));
    }
    STAMP(2);
    { const unsigned vg = norm(xg0); if (lane < 40) ((LAS unsigned*)(L + L_XG))[lane] = lane < 36 ? vg : 0u; }
    if (lane < 4) ((LAS float*)(L + L_XR))[lane] = asf(xr0 << 16);
    WAIT_LGKM0();
    if (lane == 0) flags[F_XN] = 1u;
    {
      unsigned off[4]; bool live[4]; v4u g[4];
#pragma unroll
      for (int r = 0; r < 4; ++r) { off[r] = (unsigned)(WS_QP + (kvh * 4 + r) * 1024 + lane * 16); live[r] = true; }
      __builtin_amdgcn_s_sleep(DLY_Q);
      wait_until(t_start + T_Q - UNI(flags[W_SHIFT]));
      poll<4>(rws, off, live, tag, g);
      const f32x4 q4 = (asf4(g[0]) + asf4(g[1])) + (asf4(g[2]) + asf4(g[3]));
      LAS u16* qb = (LAS u16*)(L + L_QB) + 64 * (lane >> 4) + 32 * (g8 & 1) + c8;
      const unsigned p01 = cvtpk(q4[0], q4[1]), p23 = cvtpk(q4[2], q4[3]);
      qb[0] = (u16)p01; qb[8] = (u16)(p01 >> 16); qb[16] = (u16)p23; qb[24] = (u16)(p23 >> 16);
      WAIT_LGKM0();
      if (lane == 0) flags[F_Q] = 1u;
    }
    STAMP(3);
    unsigned ooff[2];
    wo_offsets(c, 8, g8, c8, ooff);
    const unsigned poff8 = strip_off(lane, 8);
    const rsrc_t ro = mk_rsrc(p.wo, NQ * D * 2);
    int pass0 = DRY ? 0 : 1; asm volatile("" : "+s"(pass0));
#pragma nounroll
    for (int pass = pass0; pass < 2; ++pass) {
      const bool dry = pass == 0;
      if (!dry) lds_wait_slow(&flags[F_OP], 1u);
      STAMP(4);
      v4u WO[2];
      WO[0] = ld_nt(ro, dry ? OOB : ooff[0]); WO[1] = ld_nt(ro, dry ? OOB : ooff[1]);
      PIN();
      const unsigned pb = (unsigned)(WS_OP + (kvh * 16 + (lane & 15)) * 1152);
      unsigned off[2] = {pb + (4 * sp + (lane >> 4)) * 16, pb + (64 + (sp >> 3)) * 16};
      bool live[2] = {true, true}; v4u g[2];
      if (!dry) __builtin_amdgcn_s_sleep(DLY_O);
      poll<2>(rws, off, live, tag, g, dry);
      const float m = asf(g[1].x | (g[1].y >> 10)), l = asf(g[1].z | (g[1].w >> 10));
      const float M = row_max16(m);
      const float wgt = __builtin_amdgcn_exp2f(m - M);
      const float Ls = row_sum16(wgt * l);
      const float o = tr_sum16(wgt * asf4(g[0]), lane) * __builtin_amdgcn_rcpf(Ls);
      LAS unsigned* os = (LAS unsigned*)(L + (dry ? L_DUMMY : L_OS));
      if ((lane & 3) == 0) os[lane >> 2] = cvtpk(o, o);
      WAIT_LGKM0();
      STAMP(5);
      wo_partial(c, poff8, *(const LAS v2u*)((LAS unsigned char*)os + 8 * g8), WO, sel, tag, dry);
      if (lg < 18) {
        if (!dry) { lds_wait_slow(&flags[C_SP], 9u); __builtin_amdgcn_s_sleep(DLY_S); }
        const float sv = final_sum(rws, (unsigned)(WS_SP + rfin * 16), lane, tag, dry);
        if (lane < 16 && (lane & 3) == 0) ((LAS float*)(L + (dry ? L_DUMMY : L_FIN)))[lane >> 2] = sv;
        WAIT_LGKM0();
        if (lane == 0 && !dry) flags[F_S] = 1u;
        STAMP(6);
      }
    }
  } else {
    const Tag tag = mk_tag(flags[W_TAG]);
    int late;
    {
      wait_until(t_start + TB_READ);
      const v4u g = untag(ld_sc1(rws, lane < 8 ? (unsigned)(WS_TB + lane * 128) : OOB), tag);
      const bool ok = lane < 8 && (resid(g) & TMASK) == 0u;
      late = ok ? (int)(t_start - (g.x | (g.y >> 10))) : 0;
      late = late > TB_MAX ? 0 : late;
      late = max(late, __builtin_amdgcn_mov_dpp(late, XOR1, 0xf, 0xf, true));
      late = max(late, __builtin_amdgcn_mov_dpp(late, XOR2, 0xf, 0xf, true));
      late = max(late, __builtin_amdgcn_mov_dpp(late, XOR7, 0xf, 0xf, true));
      late = UNI(late);
      if (lane == 0 && late > 0) flags[W_SHIFT] = (unsigned)late;
    }
    asm volatile("" :: "v"(cpf0), "v"(cpf1), "v"(cpf2));
    const Sel sel = mk_sel(lane);
    const int g0 = 9 * (idx >> 1) + 4 * (idx & 1), odd = idx & 1;
    unsigned doff[3];
    wd_offsets(c, 8, g8, c8, doff);
    const unsigned poff8 = strip_off(lane, 8);
    const rsrc_t rd = mk_rsrc(p.wd, F * D * 2);
    const bool lc = odd && lane < 32;
    const unsigned pb = (unsigned)(WS_GP + (xg * 32 + (lane & 15)) * 2304);
    unsigned off[3] = {pb + (g0 + (lane >> 4)) * 16, pb + 16 * 2304 + (g0 + (lane >> 4)) * 16, lc ? pb + (lane >> 4) * (16 * 2304) + (g0 + 4) * 16 : OOB};
    bool live[3] = {true, true, lc}; v4u g[3];
    const int vi = (lane >> 2) & 3;
    const int col = 64 * (rfin >> 4) + 32 * ((rfin >> 3) & 1) + 8 * vi + (rfin & 7);
    const rsrc_t rout = mk_rsrc(p.out, D * 2);
    int pass0 = DRY ? 0 : 1; asm volatile("" : "+s"(pass0));
#pragma nounroll
    for (int pass = pass0; pass < 2; ++pass) {
      const bool dry = pass == 0;
      if (!dry) wait_until(t_start + T_LAST - (unsigned)late);
      v4u WD[3];
#pragma unroll
      for (int t = 0; t < 3; ++t) WD[t] = ld_nt(rd, dry ? OOB : doff[t]);
      PIN();
      if (!dry) { lds_wait(&flags[C_GU], 9u); STAMP(1); __builtin_amdgcn_s_sleep(DLY_H); }
      poll<3>(rws, off, live, tag, g, dry);
      STAMP(2);
      if (!lc) g[2] = v4u{0u, 0u, 0u, 0u};
      LAS unsigned* hs = (LAS unsigned*)(L + (dry ? L_DUMMY : L_HS));
      {
        const float gs = tr_sum16(asf4(g[0]), lane), us = tr_sum16(asf4(g[1]), lane), cs = tr_sum16(asf4(g[2]), lane);
        auto sw = __builtin_amdgcn_permlane16_swap(fbits(cs), fbits(cs), false, false);
        const bool ex = (lane & 3) == 1;
        const float gin = rbf(ex ? asf(sw[0]) : gs), uin = rbf(ex ? asf(sw[1]) : us);
        const float hv = rbf(silu_f(gin)) * uin;
        if ((lane & 3) == 0 || (ex && lane < 16)) hs[ex ? 16 + vi : lane >> 2] = cvtpk(hv, hv);
      }
      WAIT_LGKM0();
      STAMP(3);
      wd_partial(c, poff8, *(const LAS v2u*)(hs + 2 * g8), hs[16 + (g8 & 3)], WD, sel, tag, dry);
      if (lg < 18) {
        const float xr = ((const LAS float*)(L + L_XR))[vi];
        if (!dry) { lds_wait(&flags[C_FP], 9u); STAMP(4); __builtin_amdgcn_s_sleep(DLY_F); }
        const float fv = final_sum(rws, (unsigned)(WS_FP + rfin * 16), lane, tag, dry);
        STAMP(5);
        const float gate = rbf(silu_f(rbf(fv)));
        if (!dry) lds_wait(&flags[F_S], 1u);
        const float sv = ((const LAS float*)(L + L_FIN))[vi];
        const float res = xr + rbf(rbf(sv) * gate);
        __builtin_amdgcn_raw_buffer_store_b16((unsigned short)cvtpk(res, res), rout, (!dry && lane < 16 && (lane & 3) == 0) ? col * 2 : (int)OOB, 0, 0);
        if (!dry && b == 0 && lane == 0) __hip_atomic_store((GAS unsigned*)(p.ws + WS_EPOCH), flags[W_TAG], __ATOMIC_RELAXED, __HIP_MEMORY_SCOPE_AGENT);
        STAMP(6);
      }
    }
  }
  } else {
  const int st = wave - 1;
  const Sel sel = mk_sel(lane);
  const rsrc_t rg = mk_rsrc((idx >> 4) ? p.wu : p.wg, D * F * 2);
  unsigned goff, goff4, ooff[2], doff[3];
  auto late_addr = [&]() {
    PIN();
    int ln = lane; asm volatile("" : "+v"(ln));
    const int g8_ = ln >> 3, c8_ = ln & 7;
    const unsigned gcol = (unsigned)(576 * xg + 64 * st + 8 * c8_);
    goff = (unsigned)(((36 * sp + 4 * g8_) * F + gcol) * 2);
    goff4 = g8_ < 4 ? (unsigned)(((36 * sp + 32 + g8_) * F + gcol) * 2) : OOB;
    wo_offsets(c, st, g8_, c8_, ooff);
    wd_offsets(c, st, g8_, c8_, doff);
    PIN();
  };
  const unsigned poff = strip_off(lane, st);
  v4u G[5];
  unsigned tn;
#define SLOT(dt) do { wait_until(tn); tn += (dt); } while (0)
  auto issue_g = [&](int t) { SLOT(DT2); G[t] = ld_nt(rg, t < 4 ? goff + t * (F * 2) : goff4); PIN(); };
  v4u gb[5];
  auto gu_frags = [&]() {
    const v4u x4 = *(const LAS v4u*)(L + L_XG + 16 * g8);
    const unsigned x5 = ((const LAS unsigned*)(L + L_XG))[32 + g8];
#pragma unroll
    for (int t = 0; t < 4; ++t) gb[t] = sel_b(x4[t], sel);
    gb[4] = sel_b(x5, sel);
    PIN();
  };
  auto do_gu = [&](const Tag& tag) {
    f32x4 acc = {0.f, 0.f, 0.f, 0.f};
#pragma unroll
    for (int t = 0; t < 5; ++t) acc = mfma(G[t], gb[t], acc);
    publish_at(c, poff + WS_GP + (xg * 32 + idx) * 2304, acc, tag);
    if (lane == 0) lds_add(&flags[C_GU]);
    PIN();
  };

  v4u WO[2], WD[3];
  Tag tag;
  if (wave > 8) {
    tn = now32() + TSTART + 2 * DT1;
    late_addr();
#pragma unroll
    for (int t = 0; t < 5; ++t) { issue_g(t); tn += W9GAP - DT2; }
    lds_wait_slow(&flags[F_XN], 1u);
    tag = mk_tag(flags[W_TAG]);
    gu_frags();
    do_gu(tag);
    STAMP(2);
    lds_wait(&flags[C_ATT], 8u);
    STAMP(3);
    const int hh = g8 & 1;
    f32x2 ml[8]; f32x4 po[8];
#pragma unroll
    for (int w = 0; w < 8; ++w) { ml[w] = ((const LAS f32x2*)(L + L_ML))[w * 2 + hh]; po[w] = ((const LAS f32x4*)(L + L_PO))[w * 64 + lane]; }
    const float M = fmaxf(fmaxf(fmaxf(ml[0].x, ml[1].x), fmaxf(ml[2].x, ml[3].x)), fmaxf(fmaxf(ml[4].x, ml[5].x), fmaxf(ml[6].x, ml[7].x)));
    f32x4 o = {0.f, 0.f, 0.f, 0.f}; float Ls = 0.f;
#pragma unroll
    for (int w = 0; w < 8; ++w) {
      const float sc = __builtin_amdgcn_exp2f(ml[w].x - M);
      Ls = fmaf(sc, ml[w].y, Ls);
      o += sc * po[w];
    }
    const unsigned pbase = (unsigned)(WS_OP + (kvh * 16 + sp) * 1152);
    st_sc1(rws, pbase + (hh * 32 + (lane >> 4) * 8 + c8) * 16, pack4(o, tag));
    if ((lane & 0x37) == 0) st_sc1(rws, pbase + (64 + hh) * 16, pack2x(fbits(M), fbits(Ls), tag));
    if (lane == 0) flags[F_OP] = 1u;
    PIN();
    STAMP(4);
    WO[0] = WO[1] = WD[0] = WD[1] = WD[2] = v4u{0u, 0u, 0u, 0u};
  } else {
    const size_t tile = ((size_t)kvh * SK + (SK - WIN)) * DH;
    const rsrc_t rk = mk_rsrc(p.kc + tile, WIN * DH * 2);
    const rsrc_t rv = mk_rsrc(p.vc + tile, WIN * DH * 2);
    const int cbase = (st * 8 * 16 + sp) * 1024;
    const unsigned koff = (unsigned)(((lane >> 2) & 3) * 256 + ((lane & 3) + 4 * (lane >> 4)) * 16);
    STAMP(13);
    tn = now32() + TSTART + (st * DT1) / 8;
    v4u K[8], V[8];
#define LDK(t) do { SLOT(DT1); if (t >= KWIN) WAIT_VM(KWIN - 1); K[t] = __builtin_amdgcn_raw_buffer_load_b128(rk, (int)koff, cbase + t * 16384, AUX_NT); PIN(); } while (0)
#define LDV(t) do { SLOT(DT1); WAIT_VM(KWIN - 1); V[t] = __builtin_amdgcn_raw_buffer_load_b128(rv, lane * 16, cbase + t * 16384, AUX_NT); PIN(); } while (0)
    unsigned shifted = 0;
#define CATCH_UP() do { const unsigned sh_ = UNI(flags[W_SHIFT]); tn -= sh_ - shifted; shifted = sh_; } while (0)
#pragma unroll
    for (int t = 0; t < 8; ++t) { LDK(t); if (t == 1) late_addr(); if (t == 5) CATCH_UP(); }
    constexpr float C2 = 0.08838834764831845f * 1.4426950408889634f;
    unsigned pk[8];
    auto qk_softmax = [&]() {
      const v4u qf = *(const LAS v4u*)(L + L_QB + ((g8 & 1) * 128 + ((lane & 3) + 4 * (lane >> 4)) * 8) * 2);
      float s[8];
#pragma unroll
      for (int t = 0; t < 8; ++t) {
        const f32x4 d = mfma(K[t], qf, f32x4{0.f, 0.f, 0.f, 0.f});
        s[t] = (dpp<0x00>(d[0]) + dpp<0x55>(d[1])) + (dpp<0xAA>(d[2]) + dpp<0xFF>(d[3]));
      }
      float m = fmaxf(fmaxf(fmaxf(s[0], s[1]), fmaxf(s[2], s[3])), fmaxf(fmaxf(s[4], s[5]), fmaxf(s[6], s[7])));
      m = x16_max(m) * C2;
      float l = 0.f;
#pragma unroll
      for (int t = 0; t < 8; ++t) { const float pr = __builtin_amdgcn_exp2f(fmaf(s[t], C2, -m)); l += pr; pk[t] = cvtpk(pr, pr); }
      l = x16_sum(l);
      if ((lane & 0x37) == 0) ((LAS f32x2*)(L + L_ML))[st * 2 + g8] = f32x2{m, l};
      PIN();
    };
    auto pv = [&]() {
      f32x4 oacc = {0.f, 0.f, 0.f, 0.f};
#pragma unroll
      for (int t = 0; t < 8; ++t) oacc = mfma(V[t], sel_b(pk[t], sel), oacc);
      ((LAS f32x4*)(L + L_PO))[st * 64 + lane] = oacc;
      WAIT_LGKM0();
      if (lane == 0) lds_add(&flags[C_ATT]);
      PIN();
    };
#pragma unroll
    for (int t = 0; t < 8; ++t) { LDV(t); if (t == 0 || t == 3) CATCH_UP(); }
    STAMP(11);
    tn -= (st * DT1) / 8;
    bool early = UNI(flags[F_Q]) != 0u;
    if (early) qk_softmax();
    STAMP(3);
    issue_g(0); issue_g(1);
    if (!early && UNI(flags[F_Q]) != 0u) { qk_softmax(); early = true; }
    STAMP(1);
    if (early) pv();
    STAMP(4);
    issue_g(2); issue_g(3); issue_g(4);
    if (!early) lds_wait(&flags[F_XN], 1u);
    tag = mk_tag(flags[W_TAG]);
    gu_frags();
    const rsrc_t ro = mk_rsrc(p.wo, NQ * D * 2);
    const rsrc_t rd = mk_rsrc(p.wd, F * D * 2);
    f32x4 gacc = {0.f, 0.f, 0.f, 0.f};
#pragma unroll
    for (int t = 0; t < 3; ++t) gacc = mfma(G[t], gb[t], gacc);
    PIN();
    SLOT(DT2); WO[0] = ld_nt(ro, ooff[0]); PIN();
    STAMP(5);
    gacc = mfma(G[3], gb[3], gacc);
    gacc = mfma(G[4], gb[4], gacc);
    publish_at(c, poff + WS_GP + (xg * 32 + idx) * 2304, gacc, tag);
    if (lane == 0) lds_add(&flags[C_GU]);
    PIN();
    STAMP(6);
    if (!early) { lds_wait(&flags[F_Q], 1u); qk_softmax(); pv(); }
    STAMP(2);
    SLOT(DTD); WO[1] = ld_nt(ro, ooff[1]); PIN();
    SLOT(DTD); WD[0] = ld_nt(rd, doff[0]); PIN();
    SLOT(DTD); WD[1] = ld_nt(rd, doff[1]); PIN();
    if (idx & 1) SLOT(DTD);
    WD[2] = ld_nt(rd, doff[2]); PIN();
  }
  {
    const bool dry = wave > 8;
    v2u h2, o2; unsigned h3, spins = 0; bool hh;
#pragma nounroll
    for (;;) {
      h2 = *(const volatile LAS v2u*)(L + L_HS + 8 * g8); h3 = ((const volatile LAS unsigned*)(L + L_HS))[16 + (g8 & 3)];
      o2 = *(const volatile LAS v2u*)(L + L_OS + 8 * g8);
      hh = __all(h2.x != EMPTY && h2.y != EMPTY && h3 != EMPTY);
      if (dry || hh || __all(o2.x != EMPTY && o2.y != EMPTY) || ++spins > (SPIN_CAP << 6)) break;
    }
    int nstep = 2; asm volatile("" : "+s"(nstep));
#pragma nounroll
    for (int step = 0; step < nstep; ++step) {
      if (hh == (step == 0)) {
#pragma nounroll
        while (!dry && !__all(h2.x != EMPTY && h2.y != EMPTY && h3 != EMPTY) && ++spins <= (SPIN_CAP << 6)) {
          h2 = *(const volatile LAS v2u*)(L + L_HS + 8 * g8); h3 = ((const volatile LAS unsigned*)(L + L_HS))[16 + (g8 & 3)];
        }
        wd_partial(c, poff, h2, h3, WD, sel, tag, dry);
        STAMP(8);
      } else {
#pragma nounroll
        while (!dry && !__all(o2.x != EMPTY && o2.y != EMPTY) && ++spins <= (SPIN_CAP << 6)) o2 = *(const volatile LAS v2u*)(L + L_OS + 8 * g8);
        wo_partial(c, poff, o2, WO, sel, tag, dry);
        STAMP(7);
      }
    }
  }
  }
  STAMP_FLUSH();
}

}

extern "C" void kernel_launch(void* const* d_in, const int* in_sizes, int n_in, void* d_out, int out_size, void* d_ws, size_t ws_size, hipStream_t stream) {
  Params p;
  p.x = (const u16*)d_in[0]; p.kc = (const u16*)d_in[1]; p.vc = (const u16*)d_in[2];
  p.wq = (const u16*)d_in[3]; p.wo = (const u16*)d_in[4];
  p.wg = (const u16*)d_in[6]; p.wu = (const u16*)d_in[7]; p.wd = (const u16*)d_in[8];
  p.out = (u16*)d_out; p.ws = (unsigned char*)d_ws;
  (void)hipFuncSetAttribute((const void*)fused_decode, hipFuncAttributeMaxDynamicSharedMemorySize, LDS_BYTES);
  hipLaunchKernelGGL(fused_decode, dim3(NWG), dim3(NTHR), LDS_BYTES, stream, p);
}
```

```cpp
#include <hip/hip_runtime.h>
#include <hip/hip_bf16.h>
#include <stdint.h>

#define GAS __attribute__((address_space(1)))
#define LAS __attribute__((address_space(3)))
typedef unsigned v4u __attribute__((ext_vector_type(4)));
typedef unsigned v2u __attribute__((ext_vector_type(2)));
typedef float f32x4 __attribute__((ext_vector_type(4)));
typedef float f32x2 __attribute__((ext_vector_type(2)));
typedef short bf16x8 __attribute__((ext_vector_type(8)));
typedef __bf16 bf16x2 __attribute__((ext_vector_type(2)));
typedef unsigned short u16;
typedef __amdgpu_buffer_rsrc_t rsrc_t;

namespace {

constexpr int KWIN = 4;
constexpr int DT1 = 33;
constexpr int DT2 = 33;
constexpr int DTD = 24;
constexpr int DTO = 45;
constexpr int TSTART = 10;
constexpr int W9GAP = 70;
constexpr int T_Q = 450;
constexpr int T_LAST = 770;
constexpr int CODE_BYTES = 19840;
constexpr int CODE_PF_WGS = 2;
constexpr int TB_READ = 170;
constexpr int TB_MAX = 300;
constexpr int DLY_Q = 24;
constexpr int DLY_O = 30;
constexpr int DLY_H = 22;
constexpr int DLY_F = 22;
constexpr int DLY_S = 28;
constexpr int RETRY = 8;

constexpr int D = 576, SK = 65536, DH = 128, WIN = 4096, F = 4608, NQ = 4096;
constexpr int NWG = 256, NWAVE = 11, NTHR = NWAVE * 64;
constexpr int LDS_BYTES = 84 * 1024;

constexpr int WS_EPOCH = 0;
constexpr int WS_TB = 256;
constexpr int WS_QP = WS_TB + 1024;
constexpr int WS_OP = WS_QP + 16 * 4096;
constexpr int WS_GP = WS_OP + 16 * 16 * 1152;
constexpr int WS_SP = WS_GP + 8 * 32 * 2304;
constexpr int WS_FP = WS_SP + 256 * 2304;
constexpr int WS_END = WS_FP + 256 * 2304;

constexpr int L_FLAG = 0;
constexpr int L_OS = 64;
constexpr int L_HS = L_OS + 64;
constexpr int L_XQ = L_HS + 128;
constexpr int L_XG = L_XQ + 576;
constexpr int L_XR = L_XG + 160;
constexpr int L_QB = L_XR + 16;
constexpr int L_ML = L_QB + 512;
constexpr int L_FIN = L_ML + 128;
constexpr int L_PO = L_FIN + 16;
constexpr int L_X = L_PO + 8192;
constexpr int L_WQ = L_X + 2048;
enum { F_XN = 0, F_Q = 1, C_ATT = 2, C_GU = 3, F_OP = 4, C_SP = 5, C_FP = 6, F_S = 7, W_TAG = 8, W_SHIFT = 9, F_X = 10, F_WQ = 11 };

constexpr int AUX_NT = 2, AUX_SC1 = 16;
constexpr unsigned VMASK = 0xFFFFFC00u, TMASK = 0x3FFu, OOB = 0xFFFFFF00u;
constexpr unsigned EMPTY = 0xFFFF0000u;
constexpr unsigned SPIN_CAP = 1u << 17;

struct Params {
  const u16* x; const u16* kc; const u16* vc; const u16* wq; const u16* wo;
  const u16* wg; const u16* wu; const u16* wd; u16* out; unsigned char* ws;
};

__device__ __forceinline__ unsigned fbits(float f) { return __builtin_bit_cast(unsigned, f); }
__device__ __forceinline__ float asf(unsigned u) { return __builtin_bit_cast(float, u); }
__device__ __forceinline__ unsigned cvtpk(float a, float b) { bf16x2 r = {(__bf16)a, (__bf16)b}; return __builtin_bit_cast(unsigned, r); }
__device__ __forceinline__ float rbf(float f) { return asf(cvtpk(0.f, f) & 0xffff0000u); }
__device__ __forceinline__ float silu_f(float x) { return x * __builtin_amdgcn_rcpf(1.f + __builtin_amdgcn_exp2f(x * -1.4426950408889634f)); }
__device__ __forceinline__ f32x4 mfma(v4u a, v4u b, f32x4 c) {
  return __builtin_amdgcn_mfma_f32_16x16x32_bf16(__builtin_bit_cast(bf16x8, a), __builtin_bit_cast(bf16x8, b), c, 0, 0, 0);
}
template <int CTRL> __device__ __forceinline__ float dpp(float x) {
  return asf(__builtin_amdgcn_mov_dpp(__builtin_bit_cast(int, x), CTRL, 0xf, 0xf, true));
}
template <int CTRL, int RM> __device__ __forceinline__ float dppm(float x) {
  return asf(__builtin_amdgcn_update_dpp(0, __builtin_bit_cast(int, x), CTRL, RM, 0xf, true));
}
constexpr int XOR1 = 0xB1, XOR2 = 0x4E, XOR7 = 0x141, ROR8 = 0x128;
__device__ __forceinline__ float x16_max(float x) {
  auto s = __builtin_amdgcn_permlane16_swap(fbits(x), fbits(x), false, false);
  x = fmaxf(asf(s[0]), asf(s[1]));
  auto t = __builtin_amdgcn_permlane32_swap(fbits(x), fbits(x), false, false);
  return fmaxf(asf(t[0]), asf(t[1]));
}
__device__ __forceinline__ float x16_sum(float x) {
  auto s = __builtin_amdgcn_permlane16_swap(fbits(x), fbits(x), false, false);
  x = asf(s[0]) + asf(s[1]);
  auto t = __builtin_amdgcn_permlane32_swap(fbits(x), fbits(x), false, false);
  return asf(t[0]) + asf(t[1]);
}
__device__ __forceinline__ float row_sum16(float x) { x += dpp<XOR1>(x); x += dpp<XOR2>(x); x += dpp<XOR7>(x); x += dpp<ROR8>(x); return x; }
__device__ __forceinline__ float row_max16(float x) { x = fmaxf(x, dpp<XOR1>(x)); x = fmaxf(x, dpp<XOR2>(x)); x = fmaxf(x, dpp<XOR7>(x)); x = fmaxf(x, dpp<ROR8>(x)); return x; }
__device__ __forceinline__ float wave_sum_s(float s) {
  s += dpp<0x111>(s); s += dpp<0x112>(s); s += dpp<0x114>(s); s += dpp<0x118>(s);
  s += dppm<0x142, 0xa>(s); s += dppm<0x143, 0xc>(s);
  return asf(__builtin_amdgcn_readlane(fbits(s), 63));
}
__device__ __forceinline__ float tr_sum16(f32x4 v, int lane) {
  const bool b3 = lane & 8, b2 = lane & 4;
  const float a = (b3 ? v[2] : v[0]) + dpp<ROR8>(b3 ? v[0] : v[2]);
  const float b = (b3 ? v[3] : v[1]) + dpp<ROR8>(b3 ? v[1] : v[3]);
  float r = (b2 ? b : a) + dpp<XOR7>(b2 ? a : b);
  r += dpp<XOR2>(r); r += dpp<XOR1>(r);
  return r;
}

__device__ __forceinline__ rsrc_t mk_rsrc(const void* p, unsigned bytes) {
  return __builtin_amdgcn_make_buffer_rsrc((void*)p, 0, (int)bytes, 0x00020000);
}
__device__ __forceinline__ v4u ld_nt(rsrc_t r, unsigned off) { return __builtin_amdgcn_raw_buffer_load_b128(r, (int)off, 0, AUX_NT); }
__device__ __forceinline__ v4u ld_sc1(rsrc_t r, unsigned off) { return __builtin_amdgcn_raw_buffer_load_b128(r, (int)off, 0, AUX_SC1); }
__device__ __forceinline__ void st_sc1(rsrc_t r, unsigned off, v4u g) { __builtin_amdgcn_raw_buffer_store_b128(g, r, (int)off, 0, AUX_SC1); }
__device__ __forceinline__ unsigned now32() { unsigned long long t; asm volatile("s_memrealtime %0\n\ts_waitcnt lgkmcnt(0)" : "=s"(t) :: "memory"); return (unsigned)t; }
__device__ __forceinline__ void wait_until(unsigned t) {
#pragma nounroll
  while ((int)(now32() - t) < 0) __builtin_amdgcn_s_sleep(1);
}
#define PIN() __builtin_amdgcn_sched_barrier(0)
#define CBAR() asm volatile("" ::: "memory")
#define WAIT_VM(n) asm volatile("s_waitcnt vmcnt(%0)" ::"i"(n) : "memory")
#define WAIT_LGKM0() asm volatile("s_waitcnt lgkmcnt(0)" ::: "memory")
#define UNI(x) __builtin_amdgcn_readfirstlane(x)

struct Tag { unsigned t0, t1, t2, t3; };
__device__ __forceinline__ Tag mk_tag(unsigned T) { return Tag{T & TMASK, (T + 0x155u) & TMASK, (T + 0x2AAu) & TMASK, (T + 0x0FFu) & TMASK}; }
__device__ __forceinline__ v4u pack4(f32x4 v, const Tag& t) {
  return v4u{(fbits(v.x) & VMASK) | t.t0, (fbits(v.y) & VMASK) | t.t1, (fbits(v.z) & VMASK) | t.t2, (fbits(v.w) & VMASK) | t.t3};
}
__device__ __forceinline__ v4u pack2x(unsigned a, unsigned b, const Tag& t) {
  return v4u{(a & VMASK) | t.t0, ((a & TMASK) << 10) | t.t1, (b & VMASK) | t.t2, ((b & TMASK) << 10) | t.t3};
}
__device__ __forceinline__ v4u untag(v4u g, const Tag& t) { return v4u{g.x ^ t.t0, g.y ^ t.t1, g.z ^ t.t2, g.w ^ t.t3}; }
__device__ __forceinline__ unsigned resid(v4u u) { return (u.x | u.y | u.z) | u.w; }
__device__ __forceinline__ f32x4 asf4(v4u u) { return __builtin_bit_cast(f32x4, u); }

template <int N>
__device__ __forceinline__ void poll(rsrc_t r, const unsigned (&off)[N], const bool (&live)[N], const Tag& tag, v4u (&res)[N]) {
  unsigned spins = 0;
#pragma nounroll
  for (;;) {
    v4u a[N];
#pragma unroll
    for (int i = 0; i < N; ++i) a[i] = ld_sc1(r, off[i]);
    CBAR();
    unsigned bad = 0;
#pragma unroll
    for (int i = 0; i < N; ++i) { res[i] = untag(a[i], tag); bad |= live[i] ? resid(res[i]) : 0u; }
    if (__all((bad & TMASK) == 0u) || ++spins > SPIN_CAP) break;
    __builtin_amdgcn_s_sleep(RETRY);
  }
}
__device__ __forceinline__ float final_sum(rsrc_t rws, unsigned base, int lane, const Tag& tag) {
  unsigned off[4]; bool live[4]; v4u g[4];
#pragma unroll
  for (int i = 0; i < 4; ++i) { off[i] = base + (unsigned)((i * 64 + lane) * 2304); live[i] = true; }
  poll<4>(rws, off, live, tag, g);
  const f32x4 v = (asf4(g[0]) + asf4(g[1])) + (asf4(g[2]) + asf4(g[3]));
  return x16_sum(tr_sum16(v, lane));
}

__device__ __forceinline__ void lds_wait(volatile LAS unsigned* f, unsigned want) {
  unsigned spins = 0;
#pragma nounroll
  while (*f < want) { if (++spins > (SPIN_CAP << 6)) break; }
  CBAR();
}
__device__ __forceinline__ void lds_wait_slow(volatile LAS unsigned* f, unsigned want) {
  unsigned spins = 0;
#pragma nounroll
  while (*f < want) { __builtin_amdgcn_s_sleep(2); if (++spins > (SPIN_CAP << 4)) break; }
  CBAR();
}
__device__ __forceinline__ void lds_add(volatile LAS unsigned* f) { __hip_atomic_fetch_add((LAS unsigned*)f, 1u, __ATOMIC_RELAXED, __HIP_MEMORY_SCOPE_WORKGROUP); }

struct Sel { unsigned m0, m1, m2, m3; };
__device__ __forceinline__ Sel mk_sel(int lane) {
  const unsigned h = (lane & 1) ? 0xFFFF0000u : 0x0000FFFFu; const int r = (lane & 7) >> 1;
  return Sel{r == 0 ? h : 0u, r == 1 ? h : 0u, r == 2 ? h : 0u, r == 3 ? h : 0u};
}
__device__ __forceinline__ v4u sel_b(unsigned dup, const Sel& s) { return v4u{dup & s.m0, dup & s.m1, dup & s.m2, dup & s.m3}; }
__device__ __forceinline__ f32x4 strip_fold(f32x4 d) {
  f32x4 y;
#pragma unroll
  for (int i = 0; i < 4; ++i) {
    const float t = dpp<ROR8>(d[i]);
    auto s = __builtin_amdgcn_permlane32_swap(fbits(t), fbits(t), false, false);
    y[i] = d[i] + asf(s[1]);
  }
  return y;
}

struct Ctx {
  LAS unsigned char* L; volatile LAS unsigned* flags; rsrc_t rws;
  int lane, g8, c8, b, xg, idx, kvh, sp;
};
__device__ __forceinline__ void wo_offsets(const Ctx& c, int st, int g8, int c8, unsigned (&off)[2]) {
  const int hrow = (2 * c.kvh + (c.sp >> 3)) * 128 + 32 * ((c.sp >> 1) & 3) + 4 * (c.sp & 1);
#pragma unroll
  for (int t = 0; t < 2; ++t) { const int u = 2 * g8 + t; off[t] = (unsigned)(((hrow + 8 * (u & 3) + (u >> 2)) * D + 64 * st + 8 * c8) * 2); }
}
__device__ __forceinline__ void wd_offsets(const Ctx& c, int st, int g8, int c8, unsigned (&off)[3]) {
  const int odd = c.idx & 1, g0 = 9 * (c.idx >> 1) + 4 * odd;
#pragma unroll
  for (int t = 0; t < 3; ++t) {
    const int u = t < 2 ? 2 * g8 + t : 16 + (g8 & 3), Gi = g0 + (u >> 2);
    const int row = 576 * c.xg + 64 * (Gi >> 4) + 32 * ((Gi >> 3) & 1) + 8 * (u & 3) + (Gi & 7);
    off[t] = (t < 2 || (odd && g8 < 4)) ? (unsigned)((row * D + 64 * st + 8 * c8) * 2) : OOB;
  }
}
__device__ __forceinline__ unsigned strip_off(int lane, int st) {
  unsigned o = (lane < 32 && (lane & 15) < 8) ? (unsigned)((st * 16 + (lane >> 4) * 8 + (lane & 7)) * 16) : 0x80000000u;
  asm volatile("" : "+v"(o));
  return o;
}
__device__ __forceinline__ void publish_at(const Ctx& c, unsigned off, f32x4 acc, const Tag& tag) {
  st_sc1(c.rws, off, pack4(strip_fold(acc), tag));
}
__device__ __forceinline__ void wo_partial(const Ctx& c, unsigned poff, v2u o2, const v4u (&WO)[2], const Sel& sel, const Tag& tag) {
  f32x4 acc = {0.f, 0.f, 0.f, 0.f};
  acc = mfma(WO[0], sel_b(o2.x, sel), acc);
  acc = mfma(WO[1], sel_b(o2.y, sel), acc);
  publish_at(c, poff + WS_SP + c.b * 2304, acc, tag);
  if (c.lane == 0) lds_add(&c.flags[C_SP]);
  PIN();
}
__device__ __forceinline__ void wd_partial(const Ctx& c, unsigned poff, v2u h2, unsigned h3, const v4u (&WD)[3], const Sel& sel, const Tag& tag) {
  f32x4 acc = {0.f, 0.f, 0.f, 0.f};
  acc = mfma(WD[0], sel_b(h2.x, sel), acc);
  acc = mfma(WD[1], sel_b(h2.y, sel), acc);
  acc = mfma(WD[2], sel_b(c.g8 < 4 ? h3 : 0u, sel), acc);
  publish_at(c, poff + WS_FP + c.b * 2304, acc, tag);
  if (c.lane == 0) lds_add(&c.flags[C_FP]);
  PIN();
}

__global__ void __launch_bounds__(NTHR, 1) fused_decode(Params p) {
  extern __shared__ __attribute__((aligned(16))) unsigned char smem[];
  LAS unsigned char* L = (LAS unsigned char*)smem;
  volatile LAS unsigned* flags = (volatile LAS unsigned*)(L + L_FLAG);
  const int lane = threadIdx.x & 63;
  const int wave = UNI(threadIdx.x >> 6);
  const int b = blockIdx.x, xg = b & 7, idx = b >> 3;
  const int kvh = 2 * xg + (idx >> 4), sp = idx & 15;
  const rsrc_t rws = mk_rsrc(p.ws, WS_END);
  const int lg = (idx >> 3) * 8 + xg;
  const int rfin = lg * 8 + (idx & 7);
  const int g8 = lane >> 3, c8 = lane & 7;
  const Ctx c{L, flags, rws, lane, g8, c8, b, xg, idx, kvh, sp};

  unsigned long long code_base;
  asm volatile("s_getpc_b64 %0" : "=s"(code_base));
  code_base &= ~255ull;
  unsigned cpf0 = 0, cpf1 = 0, cpf2 = 0;
  if ((threadIdx.x >> 6) == 10 && (int)(blockIdx.x >> 3) < CODE_PF_WGS) {
    const GAS unsigned* cp = (const GAS unsigned*)code_base + (threadIdx.x & 63) * 32;
    if ((threadIdx.x & 63) * 128 + 16384 < CODE_BYTES) cpf2 = cp[4096];
    if ((threadIdx.x & 63) * 128 + 8192 < CODE_BYTES) cpf1 = cp[2048];
    cpf0 = cp[0];
  }
  PIN();
  const unsigned t_start = now32();
  asm volatile("" :: "s"(p.x), "s"(p.kc), "s"(p.vc), "s"(p.wq), "s"(p.wo), "s"(p.wg), "s"(p.wu), "s"(p.wd), "s"(p.out), "s"(p.ws));
  if (wave == 0 && lane < 52) flags[lane] = lane < 16 ? 0u : EMPTY;
  WAIT_LGKM0();
  __builtin_amdgcn_s_barrier();
  PIN();

  if (wave == 10) {
    const unsigned E = __hip_atomic_load((const GAS unsigned*)(p.ws + WS_EPOCH), __ATOMIC_RELAXED, __HIP_MEMORY_SCOPE_AGENT);
    const rsrc_t rx = mk_rsrc(p.x, D * 2);
    __builtin_amdgcn_raw_ptr_buffer_load_lds(rx, (LAS void*)(L + L_X), 16, lane * 16, 0, 0, 0);
    __builtin_amdgcn_raw_ptr_buffer_load_lds(rx, (LAS void*)(L + L_X + 1024), 16, 1024 + lane * 16, 0, 0, 0);
    const rsrc_t rq = mk_rsrc(p.wq, D * NQ * 2);
    const unsigned qoff = (unsigned)(((144 * (sp >> 2) + 18 * g8) * NQ + kvh * 256 + 64 * (sp & 3) + 8 * c8) * 2);
#pragma unroll
    for (int t = 0; t < 18; ++t) __builtin_amdgcn_raw_ptr_buffer_load_lds(rq, (LAS void*)(L + L_WQ + 1024 * t), 16, qoff + t * (NQ * 2), 0, 0, AUX_NT);
    PIN();
    __builtin_amdgcn_raw_buffer_store_b16((unsigned short)0, mk_rsrc(p.out, D * 2),
                                          (lg < 18 && lane < 4) ? (64 * (rfin >> 4) + 32 * ((rfin >> 3) & 1) + 8 * lane + (rfin & 7)) * 2 : (int)OOB, 0, 0);
    PIN();
    __builtin_amdgcn_s_waitcnt(0x4F73);
    const unsigned T = UNI(E) + 1u;
    asm volatile("ds_write_b32 %0, %1 offset:%2\n\ts_waitcnt lgkmcnt(0)\n\tds_write_b32 %0, %3 offset:%4" :: "v"(0u), "v"(T), "i"(L_FLAG + 4 * W_TAG), "v"(1u), "i"(L_FLAG + 4 * F_X) : "memory");
    st_sc1(rws, (idx == 0 && lane == 0) ? (unsigned)(WS_TB + xg * 128) : OOB, pack2x(t_start, 0u, mk_tag(T)));
    __builtin_amdgcn_s_waitcnt(0x0F72);
    asm volatile("ds_write_b32 %0, %1 offset:%2" :: "v"(0u), "v"(1u), "i"(L_FLAG + 4 * F_WQ) : "memory");
  }
  unsigned wave2 = wave;
  asm volatile("" : "+s"(wave2));

  if (wave2 - 1u >= 9u) {
  if (wave2 == 0) {
    const int st = sp & 3, rs = sp >> 2;
    const Sel sel = mk_sel(lane);
    const unsigned qpoff = (lane < 32 && (lane & 15) < 8) ? (unsigned)(WS_QP + ((kvh * 4 + rs) * 4 + st) * 256 + ((lane >> 4) * 8 + c8) * 16) : OOB;
    lds_wait(&flags[F_X], 1u);
    const LAS unsigned* xl = (const LAS unsigned*)(L + L_X);
    const LAS u16* xh = (const LAS u16*)(L + L_X);
    unsigned xw[5];
#pragma unroll
    for (int i = 0; i < 4; ++i) xw[i] = xl[lane + 64 * i];
    xw[4] = lane < 32 ? xl[256 + lane] : 0u;
    const unsigned xq0 = xh[144 * rs + lane], xq1 = xh[144 * rs + 64 + lane], xq2 = xh[144 * rs + 128 + (lane & 15)];
    const unsigned xg0 = xh[36 * sp + (lane < 36 ? lane : 0)];
    const unsigned xr0 = xh[64 * (rfin >> 4) + 32 * ((rfin >> 3) & 1) + 8 * (lane & 3) + (rfin & 7)];
    const Tag tag = mk_tag(flags[W_TAG]);
    float xv[10];
#pragma unroll
    for (int i = 0; i < 5; ++i) { xv[2 * i] = asf(xw[i] << 16); xv[2 * i + 1] = asf(xw[i] & 0xffff0000u); }
    const float s = ((xv[0] + xv[1]) + (xv[2] + xv[3])) + ((xv[4] + xv[5]) + (xv[6] + xv[7])) + (xv[8] + xv[9]);
    const float mean = wave_sum_s(s) * (1.f / D);
    float q2 = 0.f, q3 = 0.f;
#pragma unroll
    for (int i = 0; i < 8; i += 2) { const float d0 = xv[i] - mean, d1 = xv[i + 1] - mean; q2 = fmaf(d0, d0, q2); q3 = fmaf(d1, d1, q3); }
    { const float d0 = lane < 32 ? xv[8] - mean : 0.f, d1 = lane < 32 ? xv[9] - mean : 0.f; q2 = fmaf(d0, d0, q2); q3 = fmaf(d1, d1, q3); }
    const float var = wave_sum_s(q2 + q3) * (1.f / D);
    const float mb = rbf(mean);
    const float den = rbf(__builtin_amdgcn_sqrtf(rbf(rbf(var) + 1e-5f)));
    const float rden = __builtin_amdgcn_rcpf(den);
    auto norm = [&](unsigned raw) -> unsigned {
      const float a = rbf(asf(raw << 16) - mb);
      float qv = a * rden; qv = fmaf(fmaf(-qv, den, a), rden, qv);
      return cvtpk(qv, qv);
    };
    LAS unsigned* xq = (LAS unsigned*)(L + L_XQ);
    xq[lane] = norm(xq0); xq[64 + lane] = norm(xq1);
    { const unsigned v2 = norm(xq2); if (lane < 16) xq[128 + lane] = v2; }
    WAIT_LGKM0();
    unsigned xv18[18];
    {
      const LAS unsigned* src = xq + 18 * g8;
#pragma unroll
      for (int i = 0; i < 9; ++i) { const v2u t2 = *(const LAS v2u*)(src + 2 * i); xv18[2 * i] = t2.x; xv18[2 * i + 1] = t2.y; }
    }
    f32x4 acc = {0.f, 0.f, 0.f, 0.f};
    lds_wait(&flags[F_WQ], 1u);
#pragma unroll
    for (int t = 0; t < 18; ++t) acc = mfma(((const LAS v4u*)(L + L_WQ))[t * 64 + lane], sel_b(xv18[t], sel), acc);
    {
      const f32x4 y = strip_fold(acc);
      st_sc1(rws, qpoff, pack4(y, tag));
    }
    { const unsigned vg = norm(xg0); if (lane < 40) ((LAS unsigned*)(L + L_XG))[lane] = lane < 36 ? vg : 0u; }
    if (lane < 4) ((LAS float*)(L + L_XR))[lane] = asf(xr0 << 16);
    WAIT_LGKM0();
    if (lane == 0) flags[F_XN] = 1u;
    {
      unsigned off[4]; bool live[4]; v4u g[4];
#pragma unroll
      for (int r = 0; r < 4; ++r) { off[r] = (unsigned)(WS_QP + (kvh * 4 + r) * 1024 + lane * 16); live[r] = true; }
      __builtin_amdgcn_s_sleep(DLY_Q);
      wait_until(t_start + T_Q - UNI(flags[W_SHIFT]));
      poll<4>(rws, off, live, tag, g);
      const f32x4 q4 = (asf4(g[0]) + asf4(g[1])) + (asf4(g[2]) + asf4(g[3]));
      LAS u16* qb = (LAS u16*)(L + L_QB) + 64 * (lane >> 4) + 32 * (g8 & 1) + c8;
      const unsigned p01 = cvtpk(q4[0], q4[1]), p23 = cvtpk(q4[2], q4[3]);
      qb[0] = (u16)p01; qb[8] = (u16)(p01 >> 16); qb[16] = (u16)p23; qb[24] = (u16)(p23 >> 16);
      WAIT_LGKM0();
      if (lane == 0) flags[F_Q] = 1u;
    }
    unsigned ooff[2];
    wo_offsets(c, 8, g8, c8, ooff);
    const unsigned poff8 = strip_off(lane, 8);
    const rsrc_t ro = mk_rsrc(p.wo, NQ * D * 2);
    lds_wait_slow(&flags[F_OP], 1u);
    v4u WO[2];
    WO[0] = ld_nt(ro, ooff[0]); WO[1] = ld_nt(ro, ooff[1]);
    PIN();
    {
      const unsigned pb = (unsigned)(WS_OP + (kvh * 16 + (lane & 15)) * 1152);
      unsigned off[2] = {pb + (4 * sp + (lane >> 4)) * 16, pb + (64 + (sp >> 3)) * 16};
      bool live[2] = {true, true}; v4u g[2];
      __builtin_amdgcn_s_sleep(DLY_O);
      poll<2>(rws, off, live, tag, g);
      const float m = asf(g[1].x | (g[1].y >> 10)), l = asf(g[1].z | (g[1].w >> 10));
      const float M = row_max16(m);
      const float wgt = __builtin_amdgcn_exp2f(m - M);
      const float Ls = row_sum16(wgt * l);
      const float o = tr_sum16(wgt * asf4(g[0]), lane) * __builtin_amdgcn_rcpf(Ls);
      if ((lane & 3) == 0) ((LAS unsigned*)(L + L_OS))[lane >> 2] = cvtpk(o, o);
      WAIT_LGKM0();
    }
    wo_partial(c, poff8, *(const LAS v2u*)(L + L_OS + 8 * g8), WO, sel, tag);
    if (lg < 18) {
      lds_wait_slow(&flags[C_SP], 9u);
      __builtin_amdgcn_s_sleep(DLY_S);
      const float sv = final_sum(rws, (unsigned)(WS_SP + rfin * 16), lane, tag);
      if (lane < 16 && (lane & 3) == 0) ((LAS float*)(L + L_FIN))[lane >> 2] = sv;
      WAIT_LGKM0();
      if (lane == 0) flags[F_S] = 1u;
    }
  } else {
    const Tag tag = mk_tag(flags[W_TAG]);
    int late;
    {
      wait_until(t_start + TB_READ);
      const v4u g = untag(ld_sc1(rws, lane < 8 ? (unsigned)(WS_TB + lane * 128) : OOB), tag);
      const bool ok = lane < 8 && (resid(g) & TMASK) == 0u;
      late = ok ? (int)(t_start - (g.x | (g.y >> 10))) : 0;
      late = late > TB_MAX ? 0 : late;
      late = max(late, __builtin_amdgcn_mov_dpp(late, XOR1, 0xf, 0xf, true));
      late = max(late, __builtin_amdgcn_mov_dpp(late, XOR2, 0xf, 0xf, true));
      late = max(late, __builtin_amdgcn_mov_dpp(late, XOR7, 0xf, 0xf, true));
      late = max(UNI(late), 0);
      if (lane == 0 && late > 0) flags[W_SHIFT] = (unsigned)late;
    }
    asm volatile("" :: "v"(cpf0), "v"(cpf1), "v"(cpf2));
    const Sel sel = mk_sel(lane);
    const int g0 = 9 * (idx >> 1) + 4 * (idx & 1), odd = idx & 1;
    unsigned doff[3];
    wd_offsets(c, 8, g8, c8, doff);
    const unsigned poff8 = strip_off(lane, 8);
    const rsrc_t rd = mk_rsrc(p.wd, F * D * 2);
    const bool lc = odd && lane < 32;
    const unsigned pb = (unsigned)(WS_GP + (xg * 32 + (lane & 15)) * 2304);
    unsigned off[3] = {pb + (g0 + (lane >> 4)) * 16, pb + 16 * 2304 + (g0 + (lane >> 4)) * 16, lc ? pb + (lane >> 4) * (16 * 2304) + (g0 + 4) * 16 : OOB};
    bool live[3] = {true, true, lc}; v4u g[3];
    const int vi = (lane >> 2) & 3;
    const int col = 64 * (rfin >> 4) + 32 * ((rfin >> 3) & 1) + 8 * vi + (rfin & 7);
    wait_until(t_start + T_LAST - (unsigned)late);
    v4u WD[3];
#pragma unroll
    for (int t = 0; t < 3; ++t) WD[t] = ld_nt(rd, doff[t]);
    PIN();
    lds_wait(&flags[C_GU], 9u);
    __builtin_amdgcn_s_sleep(DLY_H);
    poll<3>(rws, off, live, tag, g);
    if (!lc) g[2] = v4u{0u, 0u, 0u, 0u};
    {
      const float gs = tr_sum16(asf4(g[0]), lane), us = tr_sum16(asf4(g[1]), lane), cs = tr_sum16(asf4(g[2]), lane);
      auto sw = __builtin_amdgcn_permlane16_swap(fbits(cs), fbits(cs), false, false);
      const bool ex = (lane & 3) == 1;
      const float gin = rbf(ex ? asf(sw[0]) : gs), uin = rbf(ex ? asf(sw[1]) : us);
      const float hv = rbf(silu_f(gin)) * uin;
      LAS unsigned* hs = (LAS unsigned*)(L + L_HS);
      if ((lane & 3) == 0 || (ex && lane < 16)) hs[ex ? 16 + vi : lane >> 2] = cvtpk(hv, hv);
    }
    WAIT_LGKM0();
    wd_partial(c, poff8, *(const LAS v2u*)(L + L_HS + 8 * g8), ((const LAS unsigned*)(L + L_HS))[16 + (g8 & 3)], WD, sel, tag);
    if (lg < 18) {
      const float xr = ((const LAS float*)(L + L_XR))[vi];
      lds_wait(&flags[C_FP], 9u);
      __builtin_amdgcn_s_sleep(DLY_F);
      const float fv = final_sum(rws, (unsigned)(WS_FP + rfin * 16), lane, tag);
      const float gate = rbf(silu_f(rbf(fv)));
      lds_wait(&flags[F_S], 1u);
      const float sv = ((const LAS float*)(L + L_FIN))[vi];
      const float res = xr + rbf(rbf(sv) * gate);
      if (lane < 16 && (lane & 3) == 0) p.out[col] = (u16)cvtpk(res, res);
      if (b == 0 && lane == 0) __hip_atomic_store((GAS unsigned*)(p.ws + WS_EPOCH), flags[W_TAG], __ATOMIC_RELAXED, __HIP_MEMORY_SCOPE_AGENT);
    }
  }
  } else {
  const int st = wave - 1;
  const Sel sel = mk_sel(lane);
  const rsrc_t rg = mk_rsrc((idx >> 4) ? p.wu : p.wg, D * F * 2);
  unsigned goff, goff4, ooff[2], doff[3];
  auto late_addr = [&]() {
    PIN();
    int ln = lane; asm volatile("" : "+v"(ln));
    const int g8_ = ln >> 3, c8_ = ln & 7;
    const unsigned gcol = (unsigned)(576 * xg + 64 * st + 8 * c8_);
    goff = (unsigned)(((36 * sp + 4 * g8_) * F + gcol) * 2);
    goff4 = g8_ < 4 ? (unsigned)(((36 * sp + 32 + g8_) * F + gcol) * 2) : OOB;
    wo_offsets(c, st, g8_, c8_, ooff);
    wd_offsets(c, st, g8_, c8_, doff);
    PIN();
  };
  const unsigned poff = strip_off(lane, st);
  v4u G[5];
  unsigned tn;
#define SLOT(dt) do { wait_until(tn); tn += (dt); } while (0)
  auto issue_g = [&](int t) { SLOT(DT2); G[t] = ld_nt(rg, t < 4 ? goff + t * (F * 2) : goff4); PIN(); };
  v4u gb[5];
  auto gu_frags = [&]() {
    const v4u x4 = *(const LAS v4u*)(L + L_XG + 16 * g8);
    const unsigned x5 = ((const LAS unsigned*)(L + L_XG))[32 + g8];
#pragma unroll
    for (int t = 0; t < 4; ++t) gb[t] = sel_b(x4[t], sel);
    gb[4] = sel_b(x5, sel);
    PIN();
  };
  auto do_gu = [&](const Tag& tag) {
    f32x4 acc = {0.f, 0.f, 0.f, 0.f};
#pragma unroll
    for (int t = 0; t < 5; ++t) acc = mfma(G[t], gb[t], acc);
    publish_at(c, poff + WS_GP + (xg * 32 + idx) * 2304, acc, tag);
    if (lane == 0) lds_add(&flags[C_GU]);
    PIN();
  };

  if (wave > 8) {
    tn = now32() + TSTART + 2 * DT1;
    late_addr();
#pragma unroll
    for (int t = 0; t < 5; ++t) { issue_g(t); tn += W9GAP - DT2; }
    lds_wait_slow(&flags[F_XN], 1u);
    const Tag tag = mk_tag(flags[W_TAG]);
    gu_frags();
    do_gu(tag);
    lds_wait(&flags[C_ATT], 8u);
    const int hh = g8 & 1;
    f32x2 ml[8]; f32x4 po[8];
#pragma unroll
    for (int w = 0; w < 8; ++w) { ml[w] = ((const LAS f32x2*)(L + L_ML))[w * 2 + hh]; po[w] = ((const LAS f32x4*)(L + L_PO))[w * 64 + lane]; }
    const float M = fmaxf(fmaxf(fmaxf(ml[0].x, ml[1].x), fmaxf(ml[2].x, ml[3].x)), fmaxf(fmaxf(ml[4].x, ml[5].x), fmaxf(ml[6].x, ml[7].x)));
    f32x4 o = {0.f, 0.f, 0.f, 0.f}; float Ls = 0.f;
#pragma unroll
    for (int w = 0; w < 8; ++w) {
      const float sc = __builtin_amdgcn_exp2f(ml[w].x - M);
      Ls = fmaf(sc, ml[w].y, Ls);
      o += sc * po[w];
    }
    const unsigned pbase = (unsigned)(WS_OP + (kvh * 16 + sp) * 1152);
    st_sc1(rws, pbase + (hh * 32 + (lane >> 4) * 8 + c8) * 16, pack4(o, tag));
    if ((lane & 0x37) == 0) st_sc1(rws, pbase + (64 + hh) * 16, pack2x(fbits(M), fbits(Ls), tag));
    if (lane == 0) flags[F_OP] = 1u;
    PIN();
  } else {
    const size_t tile = ((size_t)kvh * SK + (SK - WIN)) * DH;
    const rsrc_t rk = mk_rsrc(p.kc + tile, WIN * DH * 2);
    const rsrc_t rv = mk_rsrc(p.vc + tile, WIN * DH * 2);
    const int cbase = (st * 8 * 16 + sp) * 1024;
    const unsigned koff = (unsigned)(((lane >> 2) & 3) * 256 + ((lane & 3) + 4 * (lane >> 4)) * 16);
    tn = now32() + TSTART + (st * DT1) / 8;
    v4u K[8], V[8];
#define LDK(t) do { SLOT(DT1); if (t >= KWIN) WAIT_VM(KWIN - 1); K[t] = __builtin_amdgcn_raw_buffer_load_b128(rk, (int)koff, cbase + t * 16384, AUX_NT); PIN(); } while (0)
#define LDV(t) do { SLOT(DT1); WAIT_VM(KWIN - 1); V[t] = __builtin_amdgcn_raw_buffer_load_b128(rv, lane * 16, cbase + t * 16384, AUX_NT); PIN(); } while (0)
    unsigned shifted = 0;
#define CATCH_UP() do { const unsigned sh_ = UNI(flags[W_SHIFT]); tn -= sh_ - shifted; shifted = sh_; } while (0)
#pragma unroll
    for (int t = 0; t < 8; ++t) { LDK(t); if (t == 1) late_addr(); if (t == 5) CATCH_UP(); }
    constexpr float C2 = 1.4426950408889634f;
    unsigned pk[8];
    auto qk_softmax = [&]() {
      const v4u qf = *(const LAS v4u*)(L + L_QB + ((g8 & 1) * 128 + ((lane & 3) + 4 * (lane >> 4)) * 8) * 2);
      float s[8];
#pragma unroll
      for (int t = 0; t < 8; ++t) {
        const f32x4 d = mfma(K[t], qf, f32x4{0.f, 0.f, 0.f, 0.f});
        s[t] = rbf(rbf((dpp<0x00>(d[0]) + dpp<0x55>(d[1])) + (dpp<0xAA>(d[2]) + dpp<0xFF>(d[3]))) * 0.08838834764831845f);
      }
      float m = fmaxf(fmaxf(fmaxf(s[0], s[1]), fmaxf(s[2], s[3])), fmaxf(fmaxf(s[4], s[5]), fmaxf(s[6], s[7])));
      m = x16_max(m) * C2;
      float l = 0.f;
#pragma unroll
      for (int t = 0; t < 8; ++t) { const float pr = __builtin_amdgcn_exp2f(fmaf(s[t], C2, -m)); l += pr; pk[t] = cvtpk(pr, pr); }
      l = x16_sum(l);
      if ((lane & 0x37) == 0) ((LAS f32x2*)(L + L_ML))[st * 2 + g8] = f32x2{m, l};
      PIN();
    };
    auto pv = [&]() {
      f32x4 oacc = {0.f, 0.f, 0.f, 0.f};
#pragma unroll
      for (int t = 0; t < 8; ++t) oacc = mfma(V[t], sel_b(pk[t], sel), oacc);
      ((LAS f32x4*)(L + L_PO))[st * 64 + lane] = oacc;
      WAIT_LGKM0();
      if (lane == 0) lds_add(&flags[C_ATT]);
      PIN();
    };
#pragma unroll
    for (int t = 0; t < 8; ++t) { LDV(t); if (t == 0 || t == 3) CATCH_UP(); }
    tn -= (st * DT1) / 8;
    bool early = UNI(flags[F_Q]) != 0u;
    if (early) qk_softmax();
    issue_g(0); issue_g(1);
    if (!early && UNI(flags[F_Q]) != 0u) { qk_softmax(); early = true; }
    if (early) pv();
    issue_g(2); issue_g(3); issue_g(4);
    if (!early) lds_wait(&flags[F_XN], 1u);
    const Tag tag = mk_tag(flags[W_TAG]);
    gu_frags();
    const rsrc_t ro = mk_rsrc(p.wo, NQ * D * 2);
    const rsrc_t rd = mk_rsrc(p.wd, F * D * 2);
    v4u WO[2], WD[3];
    f32x4 gacc = {0.f, 0.f, 0.f, 0.f};
#pragma unroll
    for (int t = 0; t < 3; ++t) gacc = mfma(G[t], gb[t], gacc);
    PIN();
    tn += DTD - DT2;
    SLOT(DTD); WD[0] = ld_nt(rd, doff[0]); PIN();
    gacc = mfma(G[3], gb[3], gacc);
    gacc = mfma(G[4], gb[4], gacc);
    publish_at(c, poff + WS_GP + (xg * 32 + idx) * 2304, gacc, tag);
    if (lane == 0) lds_add(&flags[C_GU]);
    PIN();
    if (!early) { lds_wait(&flags[F_Q], 1u); qk_softmax(); pv(); }
    SLOT(DTD); WD[1] = ld_nt(rd, doff[1]); PIN();
    if (idx & 1) SLOT(DTD);
    WD[2] = ld_nt(rd, doff[2]); PIN();
    tn += DTO - DTD;
    SLOT(DT2); WO[0] = ld_nt(ro, ooff[0]); PIN();
    SLOT(DT2); WO[1] = ld_nt(ro, ooff[1]); PIN();
    v2u h2, o2; unsigned h3, spins = 0; bool hh;
#pragma nounroll
    for (;;) {
      h2 = *(const volatile LAS v2u*)(L + L_HS + 8 * g8); h3 = ((const volatile LAS unsigned*)(L + L_HS))[16 + (g8 & 3)];
      o2 = *(const volatile LAS v2u*)(L + L_OS + 8 * g8);
      hh = __all(h2.x != EMPTY && h2.y != EMPTY && h3 != EMPTY);
      if (hh || __all(o2.x != EMPTY && o2.y != EMPTY) || ++spins > (SPIN_CAP << 6)) break;
    }
    if (hh) {
      wd_partial(c, poff, h2, h3, WD, sel, tag);
#pragma nounroll
      while (!__all(o2.x != EMPTY && o2.y != EMPTY) && ++spins <= (SPIN_CAP << 6)) o2 = *(const volatile LAS v2u*)(L + L_OS + 8 * g8);
      wo_partial(c, poff, o2, WO, sel, tag);
    } else {
      wo_partial(c, poff, o2, WO, sel, tag);
#pragma nounroll
      while (!__all(h2.x != EMPTY && h2.y != EMPTY && h3 != EMPTY) && ++spins <= (SPIN_CAP << 6)) {
        h2 = *(const volatile LAS v2u*)(L + L_HS + 8 * g8); h3 = ((const volatile LAS unsigned*)(L + L_HS))[16 + (g8 & 3)];
      }
      wd_partial(c, poff, h2, h3, WD, sel, tag);
    }
  }
  }
}

}

extern "C" void kernel_launch(void* const* d_in, const int* in_sizes, int n_in, void* d_out, int out_size, void* d_ws, size_t ws_size, hipStream_t stream) {
  Params p;
  p.x = (const u16*)d_in[0]; p.kc = (const u16*)d_in[1]; p.vc = (const u16*)d_in[2];
  p.wq = (const u16*)d_in[3]; p.wo = (const u16*)d_in[4];
  p.wg = (const u16*)d_in[6]; p.wu = (const u16*)d_in[7]; p.wd = (const u16*)d_in[8];
  p.out = (u16*)d_out; p.ws = (unsigned char*)d_ws;
  (void)hipFuncSetAttribute((const void*)fused_decode, hipFuncAttributeMaxDynamicSharedMemorySize, LDS_BYTES);
  hipLaunchKernelGGL(fused_decode, dim3(NWG), dim3(NTHR), LDS_BYTES, stream, p);
}
```
